```python
import torch

def _softmax(x, axis=-1):
    return torch.softmax(x, dim=axis)
def _gelu(x):
    return torch.nn.functional.gelu(x, approximate="tanh")
def _silu(x):
    return torch.nn.functional.silu(x)
def _sigmoid(x):
    return torch.sigmoid(x)
def _layernorm(x, axis=-1, eps=1e-5):
    m = x.mean(dim=axis, keepdim=True)
    v = x.var(dim=axis, keepdim=True, unbiased=False)
    return (x - m) / torch.sqrt(v + eps)
def _scatter_add(tgt, idx, src, axis=0):
    return tgt.clone().index_add_(axis, idx, src)
def _segment_sum(src, seg, n):
    out = torch.zeros((n,) + src.shape[1:], dtype=src.dtype)
    return out.index_add_(0, seg, src)
def _pad(x, pw):
    flat = tuple(v for pair in reversed(pw) for v in pair)
    return torch.nn.functional.pad(x, flat)

kernel_name = 'standalone__mha_softmax__near_ridge'

def setup_inputs():

    _w = (lambda a: (lambda t: t.to(torch.bfloat16) if t.is_floating_point() else t)(torch.as_tensor(a)))
    B, H, Sq, Sk, D = 8, 16, 384, 1024, 256
    return {
        "q": _w((lambda *s: torch.randn(*s, dtype=torch.bfloat16) * 0.8)(B, H, Sq, D)),
        "k": _w((lambda *s: torch.randn(*s, dtype=torch.bfloat16) * 0.8)(B, H, Sk, D)),
        "v": _w((lambda *s: torch.randn(*s, dtype=torch.bfloat16) * 0.05)(B, H, Sk, D)),
    }

def reference(q, k, v):
    scale = q.shape[-1] ** -0.5
    scores = torch.einsum("bhqd,bhkd->bhqk", q, k) * scale
    p = _softmax(scores, axis=-1)
    return torch.einsum("bhqk,bhkd->bhqd", p, v)
```

```cpp
#include <hip/hip_runtime.h>
#include <hip/hip_bf16.h>
#include <stdint.h>

typedef __attribute__((ext_vector_type(8))) short bf16x8;
typedef __attribute__((ext_vector_type(4))) short s16x4;
typedef __attribute__((ext_vector_type(4))) float f32x4;
typedef __attribute__((ext_vector_type(4))) unsigned u32x4;
typedef __attribute__((ext_vector_type(2))) unsigned u32x2;
typedef short v4i16_t __attribute__((ext_vector_type(4)));
typedef __attribute__((address_space(3))) const char* lds_cptr;

constexpr int NB = 8, NH = 16, SQ = 384, SK = 1024, D = 256;
constexpr int NT = SK / 64;
constexpr unsigned KS0 = 0, VS0 = 65536;
constexpr int LDS_BYTES = 131072;
constexpr float CSC = 0.0625f * 1.4426950408889634f;
constexpr float THR = 8.0f / 0.0625f;

#define DI __device__ __forceinline__
#define SBAR() __builtin_amdgcn_sched_barrier(0)
#define PIN(x) asm volatile("" : "+v"(x))

typedef float f32x2_ __attribute__((ext_vector_type(2))); typedef __bf16 bf16x2_ __attribute__((ext_vector_type(2)));
DI unsigned cvtpk(float lo, float hi) { f32x2_ v = {lo, hi}; bf16x2_ b = __builtin_convertvector(v, bf16x2_); return __builtin_bit_cast(unsigned, b); }
DI float max3f(float a, float b, float c) { float r; asm("v_max3_f32 %0, %1, %2, %3" : "=v"(r) : "v"(a), "v"(b), "v"(c)); return r; }
DI float max2f(float a, float b) { float r; asm("v_max_f32_e32 %0, %1, %2" : "=v"(r) : "v"(a), "v"(b)); return r; }
DI float fadd_s(float a, float b) { return a + b; }
DI float ffma_s(float a, float b, float c) { return __builtin_fmaf(a, b, c); }
DI float exp_s(float a) { return __builtin_amdgcn_exp2f(a); }
DI void glds16(unsigned voff, const void* sbase, unsigned lds_dst) {
  asm volatile("s_mov_b32 m0, %2\n\ts_nop 0\n\tglobal_load_lds_dwordx4 %0, %1" :: "v"(voff), "s"(sbase), "s"(lds_dst) : "memory");
}
DI float xmax16(float x) { auto r = __builtin_amdgcn_permlane16_swap(__float_as_uint(x), __float_as_uint(x), false, false);
  return max2f(__uint_as_float(r[0]), __uint_as_float(r[1])); }
DI float xmax32(float x) { auto r = __builtin_amdgcn_permlane32_swap(__float_as_uint(x), __float_as_uint(x), false, false);
  return max2f(__uint_as_float(r[0]), __uint_as_float(r[1])); }
DI float xadd16(float x) { auto r = __builtin_amdgcn_permlane16_swap(__float_as_uint(x), __float_as_uint(x), false, false);
  return __uint_as_float(r[0]) + __uint_as_float(r[1]); }
DI float xadd32(float x) { auto r = __builtin_amdgcn_permlane32_swap(__float_as_uint(x), __float_as_uint(x), false, false);
  return __uint_as_float(r[0]) + __uint_as_float(r[1]); }
#define ACLOB "a0","a1","a2","a3","a4","a5","a6","a7","a8","a9","a10","a11","a12","a13","a14","a15","a16","a17","a18","a19","a20","a21","a22","a23","a24","a25","a26","a27","a28","a29","a30","a31","a32","a33","a34","a35","a36","a37","a38","a39","a40","a41","a42","a43","a44","a45","a46","a47","a48","a49","a50","a51","a52","a53","a54","a55","a56","a57","a58","a59","a60","a61","a62","a63","a64","a65","a66","a67","a68","a69","a70","a71","a72","a73","a74","a75","a76","a77","a78","a79","a80","a81","a82","a83","a84","a85","a86","a87","a88","a89","a90","a91","a92","a93","a94","a95","a96","a97","a98","a99","a100","a101","a102","a103","a104","a105","a106","a107","a108","a109","a110","a111","a112","a113","a114","a115","a116","a117","a118","a119","a120","a121","a122","a123","a124","a125","a126","a127","a128","a129","a130","a131","a132","a133","a134","a135","a136","a137","a138","a139","a140","a141","a142","a143","a144","a145","a146","a147","a148","a149","a150","a151","a152","a153","a154","a155","a156","a157","a158","a159","a160","a161","a162","a163","a164","a165","a166","a167","a168","a169","a170","a171","a172","a173","a174","a175","a176","a177","a178","a179","a180","a181","a182","a183","a184","a185","a186","a187","a188","a189","a190","a191","a192","a193","a194","a195","a196","a197","a198","a199","a200","a201","a202","a203","a204","a205","a206","a207","a208","a209","a210","a211","a212","a213","a214","a215","a216","a217","a218","a219","a220","a221","a222","a223","a224","a225","a226","a227","a228","a229","a230","a231","a232","a233","a234","a235","a236","a237","a238","a239","a240","a241","a242","a243","a244","a245","a246","a247","a248","a249","a250","a251","a252","a253","a254","a255"
#define MF_S0V(S, KF, QF) asm volatile("v_mfma_f32_16x16x32_bf16 %0, %1, %2, 0" : "=v"(S) : "v"(KF), "v"(QF) : ACLOB)
#define MF_SV(S, KF, QF)  asm volatile("v_mfma_f32_16x16x32_bf16 %0, %1, %2, %0" : "+v"(S) : "v"(KF), "v"(QF) : ACLOB)
#define MF_S0A(S, KF, A0) asm volatile("v_mfma_f32_16x16x32_bf16 %0, %1, a[%c2:%c3], 0" : "=v"(S) : "v"(KF), "i"(A0), "i"(A0 + 3) : ACLOB)
#define MF_SA(S, KF, A0)  asm volatile("v_mfma_f32_16x16x32_bf16 %0, %1, a[%c2:%c3], %0" : "+v"(S) : "v"(KF), "i"(A0), "i"(A0 + 3) : ACLOB)
#define MF_O(A0, VF, PF)  asm volatile("v_mfma_f32_16x16x32_bf16 a[%c2:%c3], %0, %1, a[%c2:%c3]" :: "v"(VF), "v"(PF), "i"(A0), "i"(A0 + 3) : ACLOB)
#define MF_S3(Z, S0_, S1_, S2_, KF, QF, A0, A1) asm volatile("v_mfma_f32_16x16x32_bf16 %0, %3, a[%c5:%c6], " Z "\n\tv_mfma_f32_16x16x32_bf16 %1, %3, a[%c7:%c8], " Z2(Z,1) "\n\tv_mfma_f32_16x16x32_bf16 %2, %3, %4, " Z2(Z,2) : "+v"(S0_), "+v"(S1_), "+v"(S2_) : "v"(KF), "v"(QF), "i"(A0), "i"(A0 + 3), "i"(A1), "i"(A1 + 3) : ACLOB)
#define MF_S3C(S0_, S1_, S2_, KF, QF, A0, A1) asm volatile("v_mfma_f32_16x16x32_bf16 %0, %3, a[%c5:%c6], %0\n\tv_mfma_f32_16x16x32_bf16 %1, %3, a[%c7:%c8], %1\n\tv_mfma_f32_16x16x32_bf16 %2, %3, %4, %2" : "+v"(S0_), "+v"(S1_), "+v"(S2_) : "v"(KF), "v"(QF), "i"(A0), "i"(A0 + 3), "i"(A1), "i"(A1 + 3) : ACLOB)
#define MF_S3Z(S0_, S1_, S2_, KF, QF, A0, A1) asm volatile("v_mfma_f32_16x16x32_bf16 %0, %3, a[%c5:%c6], 0\n\tv_mfma_f32_16x16x32_bf16 %1, %3, a[%c7:%c8], 0\n\tv_mfma_f32_16x16x32_bf16 %2, %3, %4, 0" : "=&v"(S0_), "=&v"(S1_), "=&v"(S2_) : "v"(KF), "v"(QF), "i"(A0), "i"(A0 + 3), "i"(A1), "i"(A1 + 3) : ACLOB)
#define MF_O3(A0, VF, P0, P1, P2) asm volatile("v_mfma_f32_16x16x32_bf16 a[%c4:%c5], %0, %1, a[%c4:%c5]\n\tv_mfma_f32_16x16x32_bf16 a[%c6:%c7], %0, %2, a[%c6:%c7]\n\tv_mfma_f32_16x16x32_bf16 a[%c8:%c9], %0, %3, a[%c8:%c9]" :: "v"(VF), "v"(P0), "v"(P1), "v"(P2), "i"(A0), "i"(A0 + 3), "i"(A0 + 64), "i"(A0 + 67), "i"(A0 + 128), "i"(A0 + 131) : ACLOB)
#define MF_S6C(S0_, S1_, S2_, KF, KG, QF, QG, A0, A1) asm volatile( \
  "v_mfma_f32_16x16x32_bf16 %0, %3, a[%c7:%c8], %0\n\tv_mfma_f32_16x16x32_bf16 %1, %3, a[%c9:%c10], %1\n\tv_mfma_f32_16x16x32_bf16 %2, %3, %5, %2\n\t" \
  "v_mfma_f32_16x16x32_bf16 %0, %4, a[%c11:%c12], %0\n\tv_mfma_f32_16x16x32_bf16 %1, %4, a[%c13:%c14], %1\n\tv_mfma_f32_16x16x32_bf16 %2, %4, %6, %2" \
  : "+v"(S0_), "+v"(S1_), "+v"(S2_) : "v"(KF), "v"(KG), "v"(QF), "v"(QG), "i"(A0), "i"(A0 + 3), "i"(A1), "i"(A1 + 3), "i"(A0 + 4), "i"(A0 + 7), "i"(A1 + 4), "i"(A1 + 7) : ACLOB)
#define MF_S6Z(S0_, S1_, S2_, KF, KG, QF, QG, A0, A1) asm volatile( \
  "v_mfma_f32_16x16x32_bf16 %0, %3, a[%c7:%c8], 0\n\tv_mfma_f32_16x16x32_bf16 %1, %3, a[%c9:%c10], 0\n\tv_mfma_f32_16x16x32_bf16 %2, %3, %5, 0\n\t" \
  "v_mfma_f32_16x16x32_bf16 %0, %4, a[%c11:%c12], %0\n\tv_mfma_f32_16x16x32_bf16 %1, %4, a[%c13:%c14], %1\n\tv_mfma_f32_16x16x32_bf16 %2, %4, %6, %2" \
  : "=&v"(S0_), "=&v"(S1_), "=&v"(S2_) : "v"(KF), "v"(KG), "v"(QF), "v"(QG), "i"(A0), "i"(A0 + 3), "i"(A1), "i"(A1 + 3), "i"(A0 + 4), "i"(A0 + 7), "i"(A1 + 4), "i"(A1 + 7) : ACLOB)
#define MF_O6(A0, VF, VG, P0, P1, P2) asm volatile( \
  "v_mfma_f32_16x16x32_bf16 a[%c5:%c6], %0, %2, a[%c5:%c6]\n\tv_mfma_f32_16x16x32_bf16 a[%c7:%c8], %0, %3, a[%c7:%c8]\n\tv_mfma_f32_16x16x32_bf16 a[%c9:%c10], %0, %4, a[%c9:%c10]\n\t" \
  "v_mfma_f32_16x16x32_bf16 a[%c11:%c12], %1, %2, a[%c11:%c12]\n\tv_mfma_f32_16x16x32_bf16 a[%c13:%c14], %1, %3, a[%c13:%c14]\n\tv_mfma_f32_16x16x32_bf16 a[%c15:%c16], %1, %4, a[%c15:%c16]" \
  :: "v"(VF), "v"(VG), "v"(P0), "v"(P1), "v"(P2), "i"(A0), "i"(A0 + 3), "i"(A0 + 64), "i"(A0 + 67), "i"(A0 + 128), "i"(A0 + 131), "i"(A0 + 4), "i"(A0 + 7), "i"(A0 + 68), "i"(A0 + 71), "i"(A0 + 132), "i"(A0 + 135) : ACLOB)
#define AWR(A, X) asm volatile("v_accvgpr_write_b32 a[%c1], %0" :: "v"(X), "i"(A) : ACLOB)
#define ARD(X, A) asm volatile("v_accvgpr_read_b32 %0, a[%c1]" : "=v"(X) : "i"(A) : ACLOB)
#define KRD(slot, kb, ks) (*(const __attribute__((address_space(3))) bf16x8*)(lds + kaddr[(ks) & 3] + (slot) * 32768 + (kb) * 8192 + ((ks) >> 2) * 256))
#define VTR(p) __builtin_bit_cast(s16x4, __builtin_amdgcn_ds_read_tr16_b64_v4i16((__attribute__((address_space(3))) v4i16_t*)(p)))
#define VRD(slot, kk, db) __builtin_shufflevector(VTR(lds + vaddr[(db) & 7] + (slot) * 32768 + (kk) * 16384 + ((db) >> 3) * 256), \
                                                  VTR(lds + vaddr[(db) & 7] + (slot) * 32768 + (kk) * 16384 + ((db) >> 3) * 256 + 8192), 0, 1, 2, 3, 4, 5, 6, 7)

__global__ __launch_bounds__(256, 1)
void attn_fwd_d256(const __hip_bfloat16* __restrict__ Q, const __hip_bfloat16* __restrict__ K,
                   const __hip_bfloat16* __restrict__ V, __hip_bfloat16* __restrict__ O) {
  extern __shared__ __attribute__((aligned(16))) char smem[];
  lds_cptr lds = (lds_cptr)smem;
  const int tid = threadIdx.x, lane = tid & 63, g = lane >> 4, c = lane & 15;
  const int w = __builtin_amdgcn_readfirstlane(tid >> 6);
  const int bid = blockIdx.x, xx = bid & 7, yy = bid >> 3;
  const int head = (yy >> 1) * 8 + xx, half = yy & 1;
  const char* Kh = (const char*)(K + (size_t)head * SK * D);
  const char* Vh = (const char*)(V + (size_t)head * SK * D);
  const int qrow0 = head * SQ + half * 192 + w * 48;

  unsigned koff[8], voff[4];
  {
    const int hi = lane >> 5, pos = lane & 31;
#pragma unroll
    for (int i = 0; i < 8; ++i) { const int row = w * 16 + 2 * i + hi; koff[i] = row * 512 + ((pos ^ (row & 15)) << 4); }
#pragma unroll
    for (int i = 0; i < 4; ++i) { const int row = w * 16 + 2 * i + hi; voff[i] = row * 512 + ((pos ^ ((row & 7) << 1)) << 4); }
  }
  const unsigned ldsw = w * 8192;
#define DMA_K1(t, slot, i) glds16(koff[i], Kh + (size_t)(t) * 32768, KS0 + (slot) * 32768 + ldsw + (i) * 1024)
#define DMA_V1(t, slot, i) glds16(voff[(i) & 3] + ((i) >> 2) * 4096, Vh + (size_t)(t) * 32768, VS0 + (slot) * 32768 + ldsw + (i) * 1024)
#define DMA_K(t, slot) do { _Pragma("unroll") for (int i = 0; i < 8; ++i) DMA_K1(t, slot, i); } while (0)
#define DMA_V(t, slot) do { _Pragma("unroll") for (int i = 0; i < 8; ++i) DMA_V1(t, slot, i); } while (0)

  bf16x8 qf2[8]; u32x4 qq[16];
  { const char* qp0 = (const char*)Q + (size_t)(qrow0 + c) * 512 + g * 16;
    asm volatile("global_load_dwordx4 %0, %1, off offset:0" : "=v"(qq[0]) : "v"(qp0 + 0));
    asm volatile("global_load_dwordx4 %0, %1, off offset:64" : "=v"(qq[1]) : "v"(qp0 + 0));
    asm volatile("global_load_dwordx4 %0, %1, off offset:128" : "=v"(qq[2]) : "v"(qp0 + 0));
    asm volatile("global_load_dwordx4 %0, %1, off offset:192" : "=v"(qq[3]) : "v"(qp0 + 0));
    asm volatile("global_load_dwordx4 %0, %1, off offset:256" : "=v"(qq[4]) : "v"(qp0 + 0));
    asm volatile("global_load_dwordx4 %0, %1, off offset:320" : "=v"(qq[5]) : "v"(qp0 + 0));
    asm volatile("global_load_dwordx4 %0, %1, off offset:384" : "=v"(qq[6]) : "v"(qp0 + 0));
    asm volatile("global_load_dwordx4 %0, %1, off offset:448" : "=v"(qq[7]) : "v"(qp0 + 0));
    asm volatile("global_load_dwordx4 %0, %1, off offset:0" : "=v"(qq[8]) : "v"(qp0 + 8192));
    asm volatile("global_load_dwordx4 %0, %1, off offset:64" : "=v"(qq[9]) : "v"(qp0 + 8192));
    asm volatile("global_load_dwordx4 %0, %1, off offset:128" : "=v"(qq[10]) : "v"(qp0 + 8192));
    asm volatile("global_load_dwordx4 %0, %1, off offset:192" : "=v"(qq[11]) : "v"(qp0 + 8192));
    asm volatile("global_load_dwordx4 %0, %1, off offset:256" : "=v"(qq[12]) : "v"(qp0 + 8192));
    asm volatile("global_load_dwordx4 %0, %1, off offset:320" : "=v"(qq[13]) : "v"(qp0 + 8192));
    asm volatile("global_load_dwordx4 %0, %1, off offset:384" : "=v"(qq[14]) : "v"(qp0 + 8192));
    asm volatile("global_load_dwordx4 %0, %1, off offset:448" : "=v"(qq[15]) : "v"(qp0 + 8192));
    asm volatile("global_load_dwordx4 %0, %1, off offset:0" : "=v"(qf2[0]) : "v"(qp0 + 16384));
    asm volatile("global_load_dwordx4 %0, %1, off offset:64" : "=v"(qf2[1]) : "v"(qp0 + 16384));
    asm volatile("global_load_dwordx4 %0, %1, off offset:128" : "=v"(qf2[2]) : "v"(qp0 + 16384));
    asm volatile("global_load_dwordx4 %0, %1, off offset:192" : "=v"(qf2[3]) : "v"(qp0 + 16384));
    asm volatile("global_load_dwordx4 %0, %1, off offset:256" : "=v"(qf2[4]) : "v"(qp0 + 16384));
    asm volatile("global_load_dwordx4 %0, %1, off offset:320" : "=v"(qf2[5]) : "v"(qp0 + 16384));
    asm volatile("global_load_dwordx4 %0, %1, off offset:384" : "=v"(qf2[6]) : "v"(qp0 + 16384));
    asm volatile("global_load_dwordx4 %0, %1, off offset:448" : "=v"(qf2[7]) : "v"(qp0 + 16384));
  }
  DMA_K(0, 0); DMA_V(0, 0); DMA_K(1, 1);
  asm volatile("s_waitcnt vmcnt(24)" : "+v"(qq[0]), "+v"(qq[1]), "+v"(qq[2]), "+v"(qq[3]), "+v"(qq[4]), "+v"(qq[5]), "+v"(qq[6]), "+v"(qq[7]), "+v"(qq[8]), "+v"(qq[9]), "+v"(qq[10]), "+v"(qq[11]), "+v"(qq[12]), "+v"(qq[13]), "+v"(qq[14]), "+v"(qq[15]) :: "memory");
  asm volatile("" : "+v"(qf2[0]), "+v"(qf2[1]), "+v"(qf2[2]), "+v"(qf2[3]), "+v"(qf2[4]), "+v"(qf2[5]), "+v"(qf2[6]), "+v"(qf2[7]));
  AWR(192, qq[0][0]); AWR(193, qq[0][1]); AWR(194, qq[0][2]); AWR(195, qq[0][3]);
  AWR(196, qq[1][0]); AWR(197, qq[1][1]); AWR(198, qq[1][2]); AWR(199, qq[1][3]);
  AWR(200, qq[2][0]); AWR(201, qq[2][1]); AWR(202, qq[2][2]); AWR(203, qq[2][3]);
  AWR(204, qq[3][0]); AWR(205, qq[3][1]); AWR(206, qq[3][2]); AWR(207, qq[3][3]);
  AWR(208, qq[4][0]); AWR(209, qq[4][1]); AWR(210, qq[4][2]); AWR(211, qq[4][3]);
  AWR(212, qq[5][0]); AWR(213, qq[5][1]); AWR(214, qq[5][2]); AWR(215, qq[5][3]);
  AWR(216, qq[6][0]); AWR(217, qq[6][1]); AWR(218, qq[6][2]); AWR(219, qq[6][3]);
  AWR(220, qq[7][0]); AWR(221, qq[7][1]); AWR(222, qq[7][2]); AWR(223, qq[7][3]);
  AWR(224, qq[8][0]); AWR(225, qq[8][1]); AWR(226, qq[8][2]); AWR(227, qq[8][3]);
  AWR(228, qq[9][0]); AWR(229, qq[9][1]); AWR(230, qq[9][2]); AWR(231, qq[9][3]);
  AWR(232, qq[10][0]); AWR(233, qq[10][1]); AWR(234, qq[10][2]); AWR(235, qq[10][3]);
  AWR(236, qq[11][0]); AWR(237, qq[11][1]); AWR(238, qq[11][2]); AWR(239, qq[11][3]);
  AWR(240, qq[12][0]); AWR(241, qq[12][1]); AWR(242, qq[12][2]); AWR(243, qq[12][3]);
  AWR(244, qq[13][0]); AWR(245, qq[13][1]); AWR(246, qq[13][2]); AWR(247, qq[13][3]);
  AWR(248, qq[14][0]); AWR(249, qq[14][1]); AWR(250, qq[14][2]); AWR(251, qq[14][3]);
  AWR(252, qq[15][0]); AWR(253, qq[15][1]); AWR(254, qq[15][2]); AWR(255, qq[15][3]);

  unsigned kaddr[4], vaddr[8];
#pragma unroll
  for (int i = 0; i < 4; ++i) kaddr[i] = c * 512 + (((4 * i + g) ^ c) << 4);
  {
    const int q4 = (lane >> 2) & 3, p4 = lane & 3, X = (4 * (g & 1) + q4) << 1;
#pragma unroll
    for (int i = 0; i < 8; ++i) vaddr[i] = VS0 + (4 * g + q4) * 512 + ((((2 * i) ^ X) + (p4 >> 1)) << 4) + 8 * (p4 & 1);
  }

  { const float z = 0.f;
    AWR(0, z);
    AWR(1, z);
    AWR(2, z);
    AWR(3, z);
    AWR(4, z);
    AWR(5, z);
    AWR(6, z);
    AWR(7, z);
    AWR(8, z);
    AWR(9, z);
    AWR(10, z);
    AWR(11, z);
    AWR(12, z);
    AWR(13, z);
    AWR(14, z);
    AWR(15, z);
    AWR(16, z);
    AWR(17, z);
    AWR(18, z);
    AWR(19, z);
    AWR(20, z);
    AWR(21, z);
    AWR(22, z);
    AWR(23, z);
    AWR(24, z);
    AWR(25, z);
    AWR(26, z);
    AWR(27, z);
    AWR(28, z);
    AWR(29, z);
    AWR(30, z);
    AWR(31, z);
    AWR(32, z);
    AWR(33, z);
    AWR(34, z);
    AWR(35, z);
    AWR(36, z);
    AWR(37, z);
    AWR(38, z);
    AWR(39, z);
    AWR(40, z);
    AWR(41, z);
    AWR(42, z);
    AWR(43, z);
    AWR(44, z);
    AWR(45, z);
    AWR(46, z);
    AWR(47, z);
    AWR(48, z);
    AWR(49, z);
    AWR(50, z);
    AWR(51, z);
    AWR(52, z);
    AWR(53, z);
    AWR(54, z);
    AWR(55, z);
    AWR(56, z);
    AWR(57, z);
    AWR(58, z);
    AWR(59, z);
    AWR(60, z);
    AWR(61, z);
    AWR(62, z);
    AWR(63, z);
    AWR(64, z);
    AWR(65, z);
    AWR(66, z);
    AWR(67, z);
    AWR(68, z);
    AWR(69, z);
    AWR(70, z);
    AWR(71, z);
    AWR(72, z);
    AWR(73, z);
    AWR(74, z);
    AWR(75, z);
    AWR(76, z);
    AWR(77, z);
    AWR(78, z);
    AWR(79, z);
    AWR(80, z);
    AWR(81, z);
    AWR(82, z);
    AWR(83, z);
    AWR(84, z);
    AWR(85, z);
    AWR(86, z);
    AWR(87, z);
    AWR(88, z);
    AWR(89, z);
    AWR(90, z);
    AWR(91, z);
    AWR(92, z);
    AWR(93, z);
    AWR(94, z);
    AWR(95, z);
    AWR(96, z);
    AWR(97, z);
    AWR(98, z);
    AWR(99, z);
    AWR(100, z);
    AWR(101, z);
    AWR(102, z);
    AWR(103, z);
    AWR(104, z);
    AWR(105, z);
    AWR(106, z);
    AWR(107, z);
    AWR(108, z);
    AWR(109, z);
    AWR(110, z);
    AWR(111, z);
    AWR(112, z);
    AWR(113, z);
    AWR(114, z);
    AWR(115, z);
    AWR(116, z);
    AWR(117, z);
    AWR(118, z);
    AWR(119, z);
    AWR(120, z);
    AWR(121, z);
    AWR(122, z);
    AWR(123, z);
    AWR(124, z);
    AWR(125, z);
    AWR(126, z);
    AWR(127, z);
    AWR(128, z);
    AWR(129, z);
    AWR(130, z);
    AWR(131, z);
    AWR(132, z);
    AWR(133, z);
    AWR(134, z);
    AWR(135, z);
    AWR(136, z);
    AWR(137, z);
    AWR(138, z);
    AWR(139, z);
    AWR(140, z);
    AWR(141, z);
    AWR(142, z);
    AWR(143, z);
    AWR(144, z);
    AWR(145, z);
    AWR(146, z);
    AWR(147, z);
    AWR(148, z);
    AWR(149, z);
    AWR(150, z);
    AWR(151, z);
    AWR(152, z);
    AWR(153, z);
    AWR(154, z);
    AWR(155, z);
    AWR(156, z);
    AWR(157, z);
    AWR(158, z);
    AWR(159, z);
    AWR(160, z);
    AWR(161, z);
    AWR(162, z);
    AWR(163, z);
    AWR(164, z);
    AWR(165, z);
    AWR(166, z);
    AWR(167, z);
    AWR(168, z);
    AWR(169, z);
    AWR(170, z);
    AWR(171, z);
    AWR(172, z);
    AWR(173, z);
    AWR(174, z);
    AWR(175, z);
    AWR(176, z);
    AWR(177, z);
    AWR(178, z);
    AWR(179, z);
    AWR(180, z);
    AWR(181, z);
    AWR(182, z);
    AWR(183, z);
    AWR(184, z);
    AWR(185, z);
    AWR(186, z);
    AWR(187, z);
    AWR(188, z);
    AWR(189, z);
    AWR(190, z);
    AWR(191, z);
  }
  float m[3], nmc[3], l[3] = {0.f, 0.f, 0.f}, mx[3];
  f32x4 sA[3][4], sB[3][4];
  bf16x8 pf[3][2], kf[8], vf[8];
  const float csc = CSC;
  u32x4 pw[3][2]; float alv[3] = {1.f, 1.f, 1.f}; int pend = 0;
  PIN(qf2[0]); PIN(qf2[1]); PIN(qf2[2]); PIN(qf2[3]); PIN(qf2[4]); PIN(qf2[5]); PIN(qf2[6]); PIN(qf2[7]);
  asm volatile("s_waitcnt vmcnt(16)\n\ts_barrier" ::: "memory");
  kf[0] = KRD(0, 0, 0); kf[1] = KRD(0, 0, 1); kf[2] = KRD(0, 0, 2); kf[3] = KRD(0, 0, 3); kf[4] = KRD(0, 0, 4); kf[5] = KRD(0, 0, 5);
  { MF_S0A(sA[0][0], kf[0], 192); kf[6] = KRD(0, 0, 6); SBAR(); }
  { MF_S0A(sA[1][0], kf[0], 224);  SBAR(); }
  { MF_S0V(sA[2][0], kf[0], qf2[0]);  SBAR(); }
  { MF_SA(sA[0][0], kf[1], 196); kf[7] = KRD(0, 0, 7); SBAR(); }
  { MF_SA(sA[1][0], kf[1], 228);  SBAR(); }
  { MF_SV(sA[2][0], kf[1], qf2[1]);  SBAR(); }
  { MF_SA(sA[0][0], kf[2], 200); kf[0] = KRD(0, 1, 0); SBAR(); }
  { MF_SA(sA[1][0], kf[2], 232);  SBAR(); }
  { MF_SV(sA[2][0], kf[2], qf2[2]);  SBAR(); }
  { MF_SA(sA[0][0], kf[3], 204); kf[1] = KRD(0, 1, 1); SBAR(); }
  { MF_SA(sA[1][0], kf[3], 236);  SBAR(); }
  { MF_SV(sA[2][0], kf[3], qf2[3]);  SBAR(); }
  { MF_SA(sA[0][0], kf[4], 208); kf[2] = KRD(0, 1, 2); SBAR(); }
  { MF_SA(sA[1][0], kf[4], 240);  SBAR(); }
  { MF_SV(sA[2][0], kf[4], qf2[4]);  SBAR(); }
  { MF_SA(sA[0][0], kf[5], 212); kf[3] = KRD(0, 1, 3); SBAR(); }
  { MF_SA(sA[1][0], kf[5], 244);  SBAR(); }
  { MF_SV(sA[2][0], kf[5], qf2[5]);  SBAR(); }
  { MF_SA(sA[0][0], kf[6], 216); kf[4] = KRD(0, 1, 4); SBAR(); }
  { MF_SA(sA[1][0], kf[6], 248);  SBAR(); }
  { MF_SV(sA[2][0], kf[6], qf2[6]);  SBAR(); }
  { MF_SA(sA[0][0], kf[7], 220); kf[5] = KRD(0, 1, 5); SBAR(); }
  { MF_SA(sA[1][0], kf[7], 252);  SBAR(); }
  { MF_SV(sA[2][0], kf[7], qf2[7]);  SBAR(); }
  { MF_S0A(sA[0][1], kf[0], 192); kf[6] = KRD(0, 1, 6); SBAR(); }
  { MF_S0A(sA[1][1], kf[0], 224);  SBAR(); }
  { MF_S0V(sA[2][1], kf[0], qf2[0]);  SBAR(); }
  { MF_SA(sA[0][1], kf[1], 196); kf[7] = KRD(0, 1, 7); SBAR(); }
  { MF_SA(sA[1][1], kf[1], 228);  SBAR(); }
  { MF_SV(sA[2][1], kf[1], qf2[1]);  SBAR(); }
  { MF_SA(sA[0][1], kf[2], 200); kf[0] = KRD(0, 2, 0); SBAR(); }
  { MF_SA(sA[1][1], kf[2], 232);  SBAR(); }
  { MF_SV(sA[2][1], kf[2], qf2[2]);  SBAR(); }
  { MF_SA(sA[0][1], kf[3], 204); kf[1] = KRD(0, 2, 1); SBAR(); }
  { MF_SA(sA[1][1], kf[3], 236);  SBAR(); }
  { MF_SV(sA[2][1], kf[3], qf2[3]);  SBAR(); }
  { MF_SA(sA[0][1], kf[4], 208); kf[2] = KRD(0, 2, 2); SBAR(); }
  { MF_SA(sA[1][1], kf[4], 240);  SBAR(); }
  { MF_SV(sA[2][1], kf[4], qf2[4]);  SBAR(); }
  { MF_SA(sA[0][1], kf[5], 212); kf[3] = KRD(0, 2, 3); SBAR(); }
  { MF_SA(sA[1][1], kf[5], 244);  SBAR(); }
  { MF_SV(sA[2][1], kf[5], qf2[5]);  SBAR(); }
  { MF_SA(sA[0][1], kf[6], 216); kf[4] = KRD(0, 2, 4); SBAR(); }
  { MF_SA(sA[1][1], kf[6], 248);  SBAR(); }
  { MF_SV(sA[2][1], kf[6], qf2[6]);  SBAR(); }
  { MF_SA(sA[0][1], kf[7], 220); kf[5] = KRD(0, 2, 5); SBAR(); }
  { MF_SA(sA[1][1], kf[7], 252);  SBAR(); }
  { MF_SV(sA[2][1], kf[7], qf2[7]);  SBAR(); }
  { MF_S0A(sA[0][2], kf[0], 192); kf[6] = KRD(0, 2, 6); SBAR(); }
  { MF_S0A(sA[1][2], kf[0], 224);  SBAR(); }
  { MF_S0V(sA[2][2], kf[0], qf2[0]);  SBAR(); }
  { MF_SA(sA[0][2], kf[1], 196); kf[7] = KRD(0, 2, 7); SBAR(); }
  { MF_SA(sA[1][2], kf[1], 228);  SBAR(); }
  { MF_SV(sA[2][2], kf[1], qf2[1]);  SBAR(); }
  { MF_SA(sA[0][2], kf[2], 200); kf[0] = KRD(0, 3, 0); SBAR(); }
  { MF_SA(sA[1][2], kf[2], 232);  SBAR(); }
  { MF_SV(sA[2][2], kf[2], qf2[2]);  SBAR(); }
  { MF_SA(sA[0][2], kf[3], 204); kf[1] = KRD(0, 3, 1); SBAR(); }
  { MF_SA(sA[1][2], kf[3], 236);  SBAR(); }
  { MF_SV(sA[2][2], kf[3], qf2[3]);  SBAR(); }
  { MF_SA(sA[0][2], kf[4], 208); kf[2] = KRD(0, 3, 2); SBAR(); }
  { MF_SA(sA[1][2], kf[4], 240);  SBAR(); }
  { MF_SV(sA[2][2], kf[4], qf2[4]);  SBAR(); }
  { MF_SA(sA[0][2], kf[5], 212); kf[3] = KRD(0, 3, 3); SBAR(); }
  { MF_SA(sA[1][2], kf[5], 244);  SBAR(); }
  { MF_SV(sA[2][2], kf[5], qf2[5]);  SBAR(); }
  { MF_SA(sA[0][2], kf[6], 216); kf[4] = KRD(0, 3, 4); SBAR(); }
  { MF_SA(sA[1][2], kf[6], 248);  SBAR(); }
  { MF_SV(sA[2][2], kf[6], qf2[6]);  SBAR(); }
  { MF_SA(sA[0][2], kf[7], 220); kf[5] = KRD(0, 3, 5); SBAR(); }
  { MF_SA(sA[1][2], kf[7], 252);  SBAR(); }
  { MF_SV(sA[2][2], kf[7], qf2[7]);  SBAR(); }
  { MF_S0A(sA[0][3], kf[0], 192); kf[6] = KRD(0, 3, 6); SBAR(); }
  { MF_S0A(sA[1][3], kf[0], 224);  SBAR(); }
  { MF_S0V(sA[2][3], kf[0], qf2[0]);  SBAR(); }
  { MF_SA(sA[0][3], kf[1], 196); kf[7] = KRD(0, 3, 7); SBAR(); }
  { MF_SA(sA[1][3], kf[1], 228);  SBAR(); }
  { MF_SV(sA[2][3], kf[1], qf2[1]);  SBAR(); }
  { MF_SA(sA[0][3], kf[2], 200);  SBAR(); }
  { MF_SA(sA[1][3], kf[2], 232);  SBAR(); }
  { MF_SV(sA[2][3], kf[2], qf2[2]);  SBAR(); }
  { MF_SA(sA[0][3], kf[3], 204);  SBAR(); }
  { MF_SA(sA[1][3], kf[3], 236);  SBAR(); }
  { MF_SV(sA[2][3], kf[3], qf2[3]);  SBAR(); }
  { MF_SA(sA[0][3], kf[4], 208);  SBAR(); }
  { MF_SA(sA[1][3], kf[4], 240);  SBAR(); }
  { MF_SV(sA[2][3], kf[4], qf2[4]);  SBAR(); }
  { MF_SA(sA[0][3], kf[5], 212);  SBAR(); }
  { MF_SA(sA[1][3], kf[5], 244);  SBAR(); }
  { MF_SV(sA[2][3], kf[5], qf2[5]);  SBAR(); }
  { MF_SA(sA[0][3], kf[6], 216);  SBAR(); }
  { MF_SA(sA[1][3], kf[6], 248);  SBAR(); }
  { MF_SV(sA[2][3], kf[6], qf2[6]);  SBAR(); }
  { MF_SA(sA[0][3], kf[7], 220);  SBAR(); }
  { MF_SA(sA[1][3], kf[7], 252);  SBAR(); }
  { MF_SV(sA[2][3], kf[7], qf2[7]);  SBAR(); }
  asm volatile("s_nop 7\n\ts_nop 7" ::: "memory");
#pragma unroll
  for (int sb = 0; sb < 3; ++sb) {
    float a = max3f(sA[sb][0][0], sA[sb][0][1], sA[sb][0][2]);
    a = max3f(a, sA[sb][0][3], sA[sb][1][0]); a = max3f(a, sA[sb][1][1], sA[sb][1][2]); a = max3f(a, sA[sb][1][3], sA[sb][2][0]);
    a = max3f(a, sA[sb][2][1], sA[sb][2][2]); a = max3f(a, sA[sb][2][3], sA[sb][3][0]); a = max3f(a, sA[sb][3][1], sA[sb][3][2]);
    a = max2f(a, sA[sb][3][3]);
    m[sb] = xmax32(xmax16(a)); nmc[sb] = -m[sb] * CSC;
  }
  asm volatile("s_barrier" ::: "memory"); DMA_K(2, 0);
  sA[0][0][0] = exp_s(ffma_s(sA[0][0][0], csc, nmc[0])); PIN(sA[0][0][0]); sA[0][0][1] = exp_s(ffma_s(sA[0][0][1], csc, nmc[0])); PIN(sA[0][0][1]); sA[0][0][2] = exp_s(ffma_s(sA[0][0][2], csc, nmc[0])); PIN(sA[0][0][2]); sA[0][0][3] = exp_s(ffma_s(sA[0][0][3], csc, nmc[0])); PIN(sA[0][0][3]); sA[0][1][0] = exp_s(ffma_s(sA[0][1][0], csc, nmc[0])); PIN(sA[0][1][0]); sA[0][1][1] = exp_s(ffma_s(sA[0][1][1], csc, nmc[0])); PIN(sA[0][1][1]); sA[0][1][2] = exp_s(ffma_s(sA[0][1][2], csc, nmc[0])); PIN(sA[0][1][2]); sA[0][1][3] = exp_s(ffma_s(sA[0][1][3], csc, nmc[0])); PIN(sA[0][1][3]); sA[0][2][0] = exp_s(ffma_s(sA[0][2][0], csc, nmc[0])); PIN(sA[0][2][0]); sA[0][2][1] = exp_s(ffma_s(sA[0][2][1], csc, nmc[0])); PIN(sA[0][2][1]); sA[0][2][2] = exp_s(ffma_s(sA[0][2][2], csc, nmc[0])); PIN(sA[0][2][2]); sA[0][2][3] = exp_s(ffma_s(sA[0][2][3], csc, nmc[0])); PIN(sA[0][2][3]); sA[0][3][0] = exp_s(ffma_s(sA[0][3][0], csc, nmc[0])); PIN(sA[0][3][0]); sA[0][3][1] = exp_s(ffma_s(sA[0][3][1], csc, nmc[0])); PIN(sA[0][3][1]); sA[0][3][2] = exp_s(ffma_s(sA[0][3][2], csc, nmc[0])); PIN(sA[0][3][2]); sA[0][3][3] = exp_s(ffma_s(sA[0][3][3], csc, nmc[0])); PIN(sA[0][3][3]); sA[1][0][0] = exp_s(ffma_s(sA[1][0][0], csc, nmc[1])); PIN(sA[1][0][0]); sA[1][0][1] = exp_s(ffma_s(sA[1][0][1], csc, nmc[1])); PIN(sA[1][0][1]); sA[1][0][2] = exp_s(ffma_s(sA[1][0][2], csc, nmc[1])); PIN(sA[1][0][2]); sA[1][0][3] = exp_s(ffma_s(sA[1][0][3], csc, nmc[1])); PIN(sA[1][0][3]); sA[1][1][0] = exp_s(ffma_s(sA[1][1][0], csc, nmc[1])); PIN(sA[1][1][0]); sA[1][1][1] = exp_s(ffma_s(sA[1][1][1], csc, nmc[1])); PIN(sA[1][1][1]); sA[1][1][2] = exp_s(ffma_s(sA[1][1][2], csc, nmc[1])); PIN(sA[1][1][2]); sA[1][1][3] = exp_s(ffma_s(sA[1][1][3], csc, nmc[1])); PIN(sA[1][1][3]); sA[1][2][0] = exp_s(ffma_s(sA[1][2][0], csc, nmc[1])); PIN(sA[1][2][0]); sA[1][2][1] = exp_s(ffma_s(sA[1][2][1], csc, nmc[1])); PIN(sA[1][2][1]); sA[1][2][2] = exp_s(ffma_s(sA[1][2][2], csc, nmc[1])); PIN(sA[1][2][2]); sA[1][2][3] = exp_s(ffma_s(sA[1][2][3], csc, nmc[1])); PIN(sA[1][2][3]); sA[1][3][0] = exp_s(ffma_s(sA[1][3][0], csc, nmc[1])); PIN(sA[1][3][0]); sA[1][3][1] = exp_s(ffma_s(sA[1][3][1], csc, nmc[1])); PIN(sA[1][3][1]); sA[1][3][2] = exp_s(ffma_s(sA[1][3][2], csc, nmc[1])); PIN(sA[1][3][2]); sA[1][3][3] = exp_s(ffma_s(sA[1][3][3], csc, nmc[1])); PIN(sA[1][3][3]); sA[2][0][0] = exp_s(ffma_s(sA[2][0][0], csc, nmc[2])); PIN(sA[2][0][0]); sA[2][0][1] = exp_s(ffma_s(sA[2][0][1], csc, nmc[2])); PIN(sA[2][0][1]); sA[2][0][2] = exp_s(ffma_s(sA[2][0][2], csc, nmc[2])); PIN(sA[2][0][2]); sA[2][0][3] = exp_s(ffma_s(sA[2][0][3], csc, nmc[2])); PIN(sA[2][0][3]); sA[2][1][0] = exp_s(ffma_s(sA[2][1][0], csc, nmc[2])); PIN(sA[2][1][0]); sA[2][1][1] = exp_s(ffma_s(sA[2][1][1], csc, nmc[2])); PIN(sA[2][1][1]); sA[2][1][2] = exp_s(ffma_s(sA[2][1][2], csc, nmc[2])); PIN(sA[2][1][2]); sA[2][1][3] = exp_s(ffma_s(sA[2][1][3], csc, nmc[2])); PIN(sA[2][1][3]);
  asm volatile("s_waitcnt vmcnt(8)" ::: "memory");
  for (int t = 0; t < NT - 4; t += 2) {
  asm volatile("s_waitcnt vmcnt(16)\n\ts_barrier" ::: "memory");
  kf[0] = KRD(1, 0, 0); kf[1] = KRD(1, 0, 1); kf[2] = KRD(1, 0, 2); kf[3] = KRD(1, 0, 3); kf[4] = KRD(1, 0, 4); kf[5] = KRD(1, 0, 5);
  { MF_S0A(sB[0][0], kf[0], 192); kf[6] = KRD(1, 0, 6); SBAR(); }
  { MF_S0A(sB[1][0], kf[0], 224); DMA_V1(t + 1, 1, 0); SBAR(); }
  { MF_S0V(sB[2][0], kf[0], qf2[0]); sA[2][2][0] = exp_s(ffma_s(sA[2][2][0], csc, nmc[2])); PIN(sA[2][2][0]); SBAR(); }
  { MF_SA(sB[0][0], kf[1], 196); kf[7] = KRD(1, 0, 7); SBAR(); }
  { MF_SA(sB[1][0], kf[1], 228); DMA_V1(t + 1, 1, 1); SBAR(); }
  { MF_SV(sB[2][0], kf[1], qf2[1]); sA[2][2][1] = exp_s(ffma_s(sA[2][2][1], csc, nmc[2])); PIN(sA[2][2][1]); SBAR(); }
  { MF_SA(sB[0][0], kf[2], 200); kf[0] = KRD(1, 1, 0); SBAR(); }
  { MF_SA(sB[1][0], kf[2], 232); DMA_V1(t + 1, 1, 2); SBAR(); }
  { MF_SV(sB[2][0], kf[2], qf2[2]); sA[2][2][2] = exp_s(ffma_s(sA[2][2][2], csc, nmc[2])); PIN(sA[2][2][2]); SBAR(); }
  { MF_SA(sB[0][0], kf[3], 204); kf[1] = KRD(1, 1, 1); SBAR(); }
  { MF_SA(sB[1][0], kf[3], 236); DMA_V1(t + 1, 1, 3); SBAR(); }
  { MF_SV(sB[2][0], kf[3], qf2[3]); sA[2][2][3] = exp_s(ffma_s(sA[2][2][3], csc, nmc[2])); PIN(sA[2][2][3]); SBAR(); }
  { MF_SA(sB[0][0], kf[4], 208); kf[2] = KRD(1, 1, 2); SBAR(); }
  { MF_SA(sB[1][0], kf[4], 240); DMA_V1(t + 1, 1, 4); SBAR(); }
  { MF_SV(sB[2][0], kf[4], qf2[4]); sA[2][3][0] = exp_s(ffma_s(sA[2][3][0], csc, nmc[2])); PIN(sA[2][3][0]); SBAR(); }
  { MF_SA(sB[0][0], kf[5], 212); kf[3] = KRD(1, 1, 3); SBAR(); }
  { MF_SA(sB[1][0], kf[5], 244); DMA_V1(t + 1, 1, 5); SBAR(); }
  { MF_SV(sB[2][0], kf[5], qf2[5]); sA[2][3][1] = exp_s(ffma_s(sA[2][3][1], csc, nmc[2])); PIN(sA[2][3][1]); SBAR(); }
  { MF_SA(sB[0][0], kf[6], 216); kf[4] = KRD(1, 1, 4); SBAR(); }
  { MF_SA(sB[1][0], kf[6], 248); DMA_V1(t + 1, 1, 6); SBAR(); }
  { MF_SV(sB[2][0], kf[6], qf2[6]); sA[2][3][2] = exp_s(ffma_s(sA[2][3][2], csc, nmc[2])); PIN(sA[2][3][2]); SBAR(); }
  { MF_SA(sB[0][0], kf[7], 220); kf[5] = KRD(1, 1, 5); SBAR(); }
  { MF_SA(sB[1][0], kf[7], 252); DMA_V1(t + 1, 1, 7); SBAR(); }
  { MF_SV(sB[2][0], kf[7], qf2[7]); sA[2][3][3] = exp_s(ffma_s(sA[2][3][3], csc, nmc[2])); PIN(sA[2][3][3]); SBAR(); }
  { MF_S0A(sB[0][1], kf[0], 192); kf[6] = KRD(1, 1, 6); SBAR(); }
  { MF_S0A(sB[1][1], kf[0], 224); l[0] = fadd_s(l[0], sA[0][0][0]); PIN(l[0]); l[0] = fadd_s(l[0], sA[0][0][1]); PIN(l[0]); SBAR(); }
  { MF_S0V(sB[2][1], kf[0], qf2[0]); pw[0][0][0] = cvtpk(sA[0][0][0], sA[0][0][1]); PIN(pw[0][0][0]); SBAR(); }
  { MF_SA(sB[0][1], kf[1], 196); kf[7] = KRD(1, 1, 7); SBAR(); }
  { MF_SA(sB[1][1], kf[1], 228); l[0] = fadd_s(l[0], sA[0][0][2]); PIN(l[0]); l[0] = fadd_s(l[0], sA[0][0][3]); PIN(l[0]); SBAR(); }
  { MF_SV(sB[2][1], kf[1], qf2[1]); pw[0][0][1] = cvtpk(sA[0][0][2], sA[0][0][3]); PIN(pw[0][0][1]); SBAR(); }
  { MF_SA(sB[0][1], kf[2], 200); kf[0] = KRD(1, 2, 0); SBAR(); }
  { MF_SA(sB[1][1], kf[2], 232); l[0] = fadd_s(l[0], sA[0][1][0]); PIN(l[0]); l[0] = fadd_s(l[0], sA[0][1][1]); PIN(l[0]); SBAR(); }
  { MF_SV(sB[2][1], kf[2], qf2[2]); pw[0][0][2] = cvtpk(sA[0][1][0], sA[0][1][1]); PIN(pw[0][0][2]); SBAR(); }
  { MF_SA(sB[0][1], kf[3], 204); kf[1] = KRD(1, 2, 1); SBAR(); }
  { MF_SA(sB[1][1], kf[3], 236); l[0] = fadd_s(l[0], sA[0][1][2]); PIN(l[0]); l[0] = fadd_s(l[0], sA[0][1][3]); PIN(l[0]); SBAR(); }
  { MF_SV(sB[2][1], kf[3], qf2[3]); pw[0][0][3] = cvtpk(sA[0][1][2], sA[0][1][3]); PIN(pw[0][0][3]); SBAR(); }
  { MF_SA(sB[0][1], kf[4], 208); kf[2] = KRD(1, 2, 2); SBAR(); }
  { MF_SA(sB[1][1], kf[4], 240); l[0] = fadd_s(l[0], sA[0][2][0]); PIN(l[0]); l[0] = fadd_s(l[0], sA[0][2][1]); PIN(l[0]); SBAR(); }
  { MF_SV(sB[2][1], kf[4], qf2[4]); pw[0][1][0] = cvtpk(sA[0][2][0], sA[0][2][1]); PIN(pw[0][1][0]); SBAR(); }
  { MF_SA(sB[0][1], kf[5], 212); kf[3] = KRD(1, 2, 3); SBAR(); }
  { MF_SA(sB[1][1], kf[5], 244); l[0] = fadd_s(l[0], sA[0][2][2]); PIN(l[0]); l[0] = fadd_s(l[0], sA[0][2][3]); PIN(l[0]); SBAR(); }
  { MF_SV(sB[2][1], kf[5], qf2[5]); pw[0][1][1] = cvtpk(sA[0][2][2], sA[0][2][3]); PIN(pw[0][1][1]); SBAR(); }
  { MF_SA(sB[0][1], kf[6], 216); kf[4] = KRD(1, 2, 4); SBAR(); }
  { MF_SA(sB[1][1], kf[6], 248); l[0] = fadd_s(l[0], sA[0][3][0]); PIN(l[0]); l[0] = fadd_s(l[0], sA[0][3][1]); PIN(l[0]); SBAR(); }
  { MF_SV(sB[2][1], kf[6], qf2[6]); pw[0][1][2] = cvtpk(sA[0][3][0], sA[0][3][1]); PIN(pw[0][1][2]); SBAR(); }
  { MF_SA(sB[0][1], kf[7], 220); kf[5] = KRD(1, 2, 5); SBAR(); }
  { MF_SA(sB[1][1], kf[7], 252); l[0] = fadd_s(l[0], sA[0][3][2]); PIN(l[0]); l[0] = fadd_s(l[0], sA[0][3][3]); PIN(l[0]); SBAR(); }
  { MF_SV(sB[2][1], kf[7], qf2[7]); pw[0][1][3] = cvtpk(sA[0][3][2], sA[0][3][3]); PIN(pw[0][1][3]); SBAR(); }
  { MF_S0A(sB[0][2], kf[0], 192); kf[6] = KRD(1, 2, 6); SBAR(); }
  { MF_S0A(sB[1][2], kf[0], 224); l[1] = fadd_s(l[1], sA[1][0][0]); PIN(l[1]); l[1] = fadd_s(l[1], sA[1][0][1]); PIN(l[1]); SBAR(); }
  { MF_S0V(sB[2][2], kf[0], qf2[0]); pw[1][0][0] = cvtpk(sA[1][0][0], sA[1][0][1]); PIN(pw[1][0][0]); SBAR(); }
  { MF_SA(sB[0][2], kf[1], 196); kf[7] = KRD(1, 2, 7); SBAR(); }
  { MF_SA(sB[1][2], kf[1], 228); l[1] = fadd_s(l[1], sA[1][0][2]); PIN(l[1]); l[1] = fadd_s(l[1], sA[1][0][3]); PIN(l[1]); SBAR(); }
  { MF_SV(sB[2][2], kf[1], qf2[1]); pw[1][0][1] = cvtpk(sA[1][0][2], sA[1][0][3]); PIN(pw[1][0][1]); SBAR(); }
  { MF_SA(sB[0][2], kf[2], 200); kf[0] = KRD(1, 3, 0); SBAR(); }
  { MF_SA(sB[1][2], kf[2], 232); l[1] = fadd_s(l[1], sA[1][1][0]); PIN(l[1]); l[1] = fadd_s(l[1], sA[1][1][1]); PIN(l[1]); SBAR(); }
  { MF_SV(sB[2][2], kf[2], qf2[2]); pw[1][0][2] = cvtpk(sA[1][1][0], sA[1][1][1]); PIN(pw[1][0][2]); SBAR(); }
  { MF_SA(sB[0][2], kf[3], 204); kf[1] = KRD(1, 3, 1); SBAR(); }
  { MF_SA(sB[1][2], kf[3], 236); l[1] = fadd_s(l[1], sA[1][1][2]); PIN(l[1]); l[1] = fadd_s(l[1], sA[1][1][3]); PIN(l[1]); SBAR(); }
  { MF_SV(sB[2][2], kf[3], qf2[3]); pw[1][0][3] = cvtpk(sA[1][1][2], sA[1][1][3]); PIN(pw[1][0][3]); SBAR(); }
  { MF_SA(sB[0][2], kf[4], 208); kf[2] = KRD(1, 3, 2); SBAR(); }
  { MF_SA(sB[1][2], kf[4], 240); l[1] = fadd_s(l[1], sA[1][2][0]); PIN(l[1]); l[1] = fadd_s(l[1], sA[1][2][1]); PIN(l[1]); SBAR(); }
  { MF_SV(sB[2][2], kf[4], qf2[4]); pw[1][1][0] = cvtpk(sA[1][2][0], sA[1][2][1]); PIN(pw[1][1][0]); SBAR(); }
  { MF_SA(sB[0][2], kf[5], 212); kf[3] = KRD(1, 3, 3); SBAR(); }
  { MF_SA(sB[1][2], kf[5], 244); l[1] = fadd_s(l[1], sA[1][2][2]); PIN(l[1]); l[1] = fadd_s(l[1], sA[1][2][3]); PIN(l[1]); SBAR(); }
  { MF_SV(sB[2][2], kf[5], qf2[5]); pw[1][1][1] = cvtpk(sA[1][2][2], sA[1][2][3]); PIN(pw[1][1][1]); SBAR(); }
  { MF_SA(sB[0][2], kf[6], 216); kf[4] = KRD(1, 3, 4); SBAR(); }
  { MF_SA(sB[1][2], kf[6], 248); l[1] = fadd_s(l[1], sA[1][3][0]); PIN(l[1]); l[1] = fadd_s(l[1], sA[1][3][1]); PIN(l[1]); SBAR(); }
  { MF_SV(sB[2][2], kf[6], qf2[6]); pw[1][1][2] = cvtpk(sA[1][3][0], sA[1][3][1]); PIN(pw[1][1][2]); SBAR(); }
  { MF_SA(sB[0][2], kf[7], 220); kf[5] = KRD(1, 3, 5); SBAR(); }
  { MF_SA(sB[1][2], kf[7], 252); l[1] = fadd_s(l[1], sA[1][3][2]); PIN(l[1]); l[1] = fadd_s(l[1], sA[1][3][3]); PIN(l[1]); SBAR(); }
  { MF_SV(sB[2][2], kf[7], qf2[7]); pw[1][1][3] = cvtpk(sA[1][3][2], sA[1][3][3]); PIN(pw[1][1][3]); SBAR(); }
  { MF_S0A(sB[0][3], kf[0], 192); kf[6] = KRD(1, 3, 6); SBAR(); }
  { MF_S0A(sB[1][3], kf[0], 224); l[2] = fadd_s(l[2], sA[2][0][0]); PIN(l[2]); l[2] = fadd_s(l[2], sA[2][0][1]); PIN(l[2]); SBAR(); }
  { MF_S0V(sB[2][3], kf[0], qf2[0]); pw[2][0][0] = cvtpk(sA[2][0][0], sA[2][0][1]); PIN(pw[2][0][0]); SBAR(); }
  { MF_SA(sB[0][3], kf[1], 196); kf[7] = KRD(1, 3, 7); SBAR(); }
  { MF_SA(sB[1][3], kf[1], 228); l[2] = fadd_s(l[2], sA[2][0][2]); PIN(l[2]); l[2] = fadd_s(l[2], sA[2][0][3]); PIN(l[2]); SBAR(); }
  { MF_SV(sB[2][3], kf[1], qf2[1]); pw[2][0][1] = cvtpk(sA[2][0][2], sA[2][0][3]); PIN(pw[2][0][1]); SBAR(); }
  { MF_SA(sB[0][3], kf[2], 200); vf[0] = VRD(0, 0, 0); SBAR(); }
  { MF_SA(sB[1][3], kf[2], 232); l[2] = fadd_s(l[2], sA[2][1][0]); PIN(l[2]); l[2] = fadd_s(l[2], sA[2][1][1]); PIN(l[2]); SBAR(); }
  { MF_SV(sB[2][3], kf[2], qf2[2]); pw[2][0][2] = cvtpk(sA[2][1][0], sA[2][1][1]); PIN(pw[2][0][2]); SBAR(); }
  { MF_SA(sB[0][3], kf[3], 204); vf[1] = VRD(0, 0, 1); SBAR(); }
  { MF_SA(sB[1][3], kf[3], 236); l[2] = fadd_s(l[2], sA[2][1][2]); PIN(l[2]); l[2] = fadd_s(l[2], sA[2][1][3]); PIN(l[2]); SBAR(); }
  { MF_SV(sB[2][3], kf[3], qf2[3]); pw[2][0][3] = cvtpk(sA[2][1][2], sA[2][1][3]); PIN(pw[2][0][3]); SBAR(); }
  { MF_SA(sB[0][3], kf[4], 208); vf[2] = VRD(0, 0, 2); SBAR(); }
  { MF_SA(sB[1][3], kf[4], 240); l[2] = fadd_s(l[2], sA[2][2][0]); PIN(l[2]); l[2] = fadd_s(l[2], sA[2][2][1]); PIN(l[2]); SBAR(); }
  { MF_SV(sB[2][3], kf[4], qf2[4]); pw[2][1][0] = cvtpk(sA[2][2][0], sA[2][2][1]); PIN(pw[2][1][0]); SBAR(); }
  { MF_SA(sB[0][3], kf[5], 212); vf[3] = VRD(0, 0, 3); SBAR(); }
  { MF_SA(sB[1][3], kf[5], 244); l[2] = fadd_s(l[2], sA[2][2][2]); PIN(l[2]); l[2] = fadd_s(l[2], sA[2][2][3]); PIN(l[2]); SBAR(); }
  { MF_SV(sB[2][3], kf[5], qf2[5]); pw[2][1][1] = cvtpk(sA[2][2][2], sA[2][2][3]); PIN(pw[2][1][1]); SBAR(); }
  { MF_SA(sB[0][3], kf[6], 216); vf[4] = VRD(0, 0, 4); SBAR(); }
  { MF_SA(sB[1][3], kf[6], 248); l[2] = fadd_s(l[2], sA[2][3][0]); PIN(l[2]); l[2] = fadd_s(l[2], sA[2][3][1]); PIN(l[2]); SBAR(); }
  { MF_SV(sB[2][3], kf[6], qf2[6]); pw[2][1][2] = cvtpk(sA[2][3][0], sA[2][3][1]); PIN(pw[2][1][2]); SBAR(); }
  { MF_SA(sB[0][3], kf[7], 220); vf[5] = VRD(0, 0, 5); SBAR(); }
  { MF_SA(sB[1][3], kf[7], 252); l[2] = fadd_s(l[2], sA[2][3][2]); PIN(l[2]); l[2] = fadd_s(l[2], sA[2][3][3]); PIN(l[2]); SBAR(); }
  { MF_SV(sB[2][3], kf[7], qf2[7]); pw[2][1][3] = cvtpk(sA[2][3][2], sA[2][3][3]); PIN(pw[2][1][3]); SBAR(); }
  _Pragma("unroll") for (int sb = 0; sb < 3; ++sb) { pf[sb][0] = __builtin_bit_cast(bf16x8, pw[sb][0]); pf[sb][1] = __builtin_bit_cast(bf16x8, pw[sb][1]); PIN(pf[sb][0]); PIN(pf[sb][1]); }
  asm volatile("s_nop 1" ::: "memory"); SBAR();
  asm volatile("s_waitcnt vmcnt(16)\n\ts_barrier" ::: "memory");
  { MF_O(0, vf[0], pf[0][0]); vf[6] = VRD(0, 0, 6); SBAR(); }
  { MF_O(64, vf[0], pf[1][0]); mx[0] = max2f(sB[0][0][0], sB[0][0][1]); PIN(mx[0]); SBAR(); }
  { MF_O(128, vf[0], pf[2][0]); mx[1] = max2f(sB[1][0][0], sB[1][0][1]); PIN(mx[1]); SBAR(); }
  { MF_O(4, vf[1], pf[0][0]); DMA_K1(t + 3, 1, 0); vf[7] = VRD(0, 0, 7); SBAR(); }
  { MF_O(68, vf[1], pf[1][0]); mx[2] = max2f(sB[2][0][0], sB[2][0][1]); PIN(mx[2]); SBAR(); }
  { MF_O(132, vf[1], pf[2][0]); mx[0] = max3f(mx[0], sB[0][0][2], sB[0][0][3]); PIN(mx[0]); SBAR(); }
  { MF_O(8, vf[2], pf[0][0]); vf[0] = VRD(0, 0, 8); SBAR(); }
  { MF_O(72, vf[2], pf[1][0]); mx[1] = max3f(mx[1], sB[1][0][2], sB[1][0][3]); PIN(mx[1]); SBAR(); }
  { MF_O(136, vf[2], pf[2][0]); mx[2] = max3f(mx[2], sB[2][0][2], sB[2][0][3]); PIN(mx[2]); SBAR(); }
  { MF_O(12, vf[3], pf[0][0]); DMA_K1(t + 3, 1, 1); vf[1] = VRD(0, 0, 9); SBAR(); }
  { MF_O(76, vf[3], pf[1][0]); mx[0] = max3f(mx[0], sB[0][1][0], sB[0][1][1]); PIN(mx[0]); SBAR(); }
  { MF_O(140, vf[3], pf[2][0]); mx[1] = max3f(mx[1], sB[1][1][0], sB[1][1][1]); PIN(mx[1]); SBAR(); }
  { MF_O(16, vf[4], pf[0][0]); vf[2] = VRD(0, 0, 10); SBAR(); }
  { MF_O(80, vf[4], pf[1][0]); mx[2] = max3f(mx[2], sB[2][1][0], sB[2][1][1]); PIN(mx[2]); SBAR(); }
  { MF_O(144, vf[4], pf[2][0]); mx[0] = max3f(mx[0], sB[0][1][2], sB[0][1][3]); PIN(mx[0]); SBAR(); }
  { MF_O(20, vf[5], pf[0][0]); DMA_K1(t + 3, 1, 2); vf[3] = VRD(0, 0, 11); SBAR(); }
  { MF_O(84, vf[5], pf[1][0]); mx[1] = max3f(mx[1], sB[1][1][2], sB[1][1][3]); PIN(mx[1]); SBAR(); }
  { MF_O(148, vf[5], pf[2][0]); mx[2] = max3f(mx[2], sB[2][1][2], sB[2][1][3]); PIN(mx[2]); SBAR(); }
  { MF_O(24, vf[6], pf[0][0]); vf[4] = VRD(0, 0, 12); SBAR(); }
  { MF_O(88, vf[6], pf[1][0]); mx[0] = max3f(mx[0], sB[0][2][0], sB[0][2][1]); PIN(mx[0]); SBAR(); }
  { MF_O(152, vf[6], pf[2][0]); mx[1] = max3f(mx[1], sB[1][2][0], sB[1][2][1]); PIN(mx[1]); SBAR(); }
  { MF_O(28, vf[7], pf[0][0]); DMA_K1(t + 3, 1, 3); vf[5] = VRD(0, 0, 13); SBAR(); }
  { MF_O(92, vf[7], pf[1][0]); mx[2] = max3f(mx[2], sB[2][2][0], sB[2][2][1]); PIN(mx[2]); SBAR(); }
  { MF_O(156, vf[7], pf[2][0]); mx[0] = max3f(mx[0], sB[0][2][2], sB[0][2][3]); PIN(mx[0]); SBAR(); }
  { MF_O(32, vf[0], pf[0][0]); vf[6] = VRD(0, 0, 14); SBAR(); }
  { MF_O(96, vf[0], pf[1][0]); mx[1] = max3f(mx[1], sB[1][2][2], sB[1][2][3]); PIN(mx[1]); SBAR(); }
  { MF_O(160, vf[0], pf[2][0]); mx[2] = max3f(mx[2], sB[2][2][2], sB[2][2][3]); PIN(mx[2]); SBAR(); }
  { MF_O(36, vf[1], pf[0][0]); DMA_K1(t + 3, 1, 4); vf[7] = VRD(0, 0, 15); SBAR(); }
  { MF_O(100, vf[1], pf[1][0]); mx[0] = max3f(mx[0], sB[0][3][0], sB[0][3][1]); PIN(mx[0]); SBAR(); }
  { MF_O(164, vf[1], pf[2][0]); mx[1] = max3f(mx[1], sB[1][3][0], sB[1][3][1]); PIN(mx[1]); SBAR(); }
  { MF_O(40, vf[2], pf[0][0]); vf[0] = VRD(0, 1, 0); SBAR(); }
  { MF_O(104, vf[2], pf[1][0]); mx[2] = max3f(mx[2], sB[2][3][0], sB[2][3][1]); PIN(mx[2]); SBAR(); }
  { MF_O(168, vf[2], pf[2][0]); mx[0] = max3f(mx[0], sB[0][3][2], sB[0][3][3]); PIN(mx[0]); SBAR(); }
  { MF_O(44, vf[3], pf[0][0]); DMA_K1(t + 3, 1, 5); vf[1] = VRD(0, 1, 1); SBAR(); }
  { MF_O(108, vf[3], pf[1][0]); mx[1] = max3f(mx[1], sB[1][3][2], sB[1][3][3]); PIN(mx[1]); SBAR(); }
  { MF_O(172, vf[3], pf[2][0]); mx[2] = max3f(mx[2], sB[2][3][2], sB[2][3][3]); PIN(mx[2]); SBAR(); }
  { MF_O(48, vf[4], pf[0][0]); vf[2] = VRD(0, 1, 2); SBAR(); }
  { MF_O(112, vf[4], pf[1][0]); mx[0] = xmax16(mx[0]); PIN(mx[0]); SBAR(); }
  { MF_O(176, vf[4], pf[2][0]); mx[1] = xmax16(mx[1]); PIN(mx[1]); SBAR(); }
  { MF_O(52, vf[5], pf[0][0]); DMA_K1(t + 3, 1, 6); vf[3] = VRD(0, 1, 3); SBAR(); }
  { MF_O(116, vf[5], pf[1][0]); mx[2] = xmax16(mx[2]); PIN(mx[2]); SBAR(); }
  { MF_O(180, vf[5], pf[2][0]); mx[0] = xmax32(mx[0]); PIN(mx[0]); SBAR(); }
  { MF_O(56, vf[6], pf[0][0]); vf[4] = VRD(0, 1, 4); SBAR(); }
  { MF_O(120, vf[6], pf[1][0]); mx[1] = xmax32(mx[1]); PIN(mx[1]); SBAR(); }
  { MF_O(184, vf[6], pf[2][0]); mx[2] = xmax32(mx[2]); PIN(mx[2]); SBAR(); }
  { MF_O(60, vf[7], pf[0][0]); DMA_K1(t + 3, 1, 7); vf[5] = VRD(0, 1, 5); SBAR(); }
  {
    const bool grow = (mx[0] - m[0] > THR) | (mx[1] - m[1] > THR) | (mx[2] - m[2] > THR);
    if (__builtin_expect(__any((int)grow), 0)) {
#pragma unroll
      for (int sb = 0; sb < 3; ++sb) { const float mn = fmaxf(m[sb], mx[sb]);
        const float al = __builtin_amdgcn_exp2f((m[sb] - mn) * CSC); m[sb] = mn; nmc[sb] = -mn * CSC; l[sb] *= al; alv[sb] = al; }
      pend = 1;
    }
    SBAR();
  }
  { MF_O(124, vf[7], pf[1][0]); sB[0][0][0] = exp_s(ffma_s(sB[0][0][0], csc, nmc[0])); PIN(sB[0][0][0]); SBAR(); }
  { MF_O(188, vf[7], pf[2][0]); sB[0][0][1] = exp_s(ffma_s(sB[0][0][1], csc, nmc[0])); PIN(sB[0][0][1]); SBAR(); }
  { MF_O(0, vf[0], pf[0][1]); vf[6] = VRD(0, 1, 6); sB[0][0][2] = exp_s(ffma_s(sB[0][0][2], csc, nmc[0])); PIN(sB[0][0][2]); SBAR(); }
  { MF_O(64, vf[0], pf[1][1]); sB[0][0][3] = exp_s(ffma_s(sB[0][0][3], csc, nmc[0])); PIN(sB[0][0][3]); SBAR(); }
  { MF_O(128, vf[0], pf[2][1]); sB[0][1][0] = exp_s(ffma_s(sB[0][1][0], csc, nmc[0])); PIN(sB[0][1][0]); SBAR(); }
  { MF_O(4, vf[1], pf[0][1]); vf[7] = VRD(0, 1, 7); sB[0][1][1] = exp_s(ffma_s(sB[0][1][1], csc, nmc[0])); PIN(sB[0][1][1]); SBAR(); }
  { MF_O(68, vf[1], pf[1][1]); sB[0][1][2] = exp_s(ffma_s(sB[0][1][2], csc, nmc[0])); PIN(sB[0][1][2]); SBAR(); }
  { MF_O(132, vf[1], pf[2][1]); sB[0][1][3] = exp_s(ffma_s(sB[0][1][3], csc, nmc[0])); PIN(sB[0][1][3]); SBAR(); }
  { MF_O(8, vf[2], pf[0][1]); vf[0] = VRD(0, 1, 8); sB[0][2][0] = exp_s(ffma_s(sB[0][2][0], csc, nmc[0])); PIN(sB[0][2][0]); SBAR(); }
  { MF_O(72, vf[2], pf[1][1]); sB[0][2][1] = exp_s(ffma_s(sB[0][2][1], csc, nmc[0])); PIN(sB[0][2][1]); SBAR(); }
  { MF_O(136, vf[2], pf[2][1]); sB[0][2][2] = exp_s(ffma_s(sB[0][2][2], csc, nmc[0])); PIN(sB[0][2][2]); SBAR(); }
  { MF_O(12, vf[3], pf[0][1]); vf[1] = VRD(0, 1, 9); sB[0][2][3] = exp_s(ffma_s(sB[0][2][3], csc, nmc[0])); PIN(sB[0][2][3]); SBAR(); }
  { MF_O(76, vf[3], pf[1][1]); sB[0][3][0] = exp_s(ffma_s(sB[0][3][0], csc, nmc[0])); PIN(sB[0][3][0]); SBAR(); }
  { MF_O(140, vf[3], pf[2][1]); sB[0][3][1] = exp_s(ffma_s(sB[0][3][1], csc, nmc[0])); PIN(sB[0][3][1]); SBAR(); }
  { MF_O(16, vf[4], pf[0][1]); vf[2] = VRD(0, 1, 10); sB[0][3][2] = exp_s(ffma_s(sB[0][3][2], csc, nmc[0])); PIN(sB[0][3][2]); SBAR(); }
  { MF_O(80, vf[4], pf[1][1]); sB[0][3][3] = exp_s(ffma_s(sB[0][3][3], csc, nmc[0])); PIN(sB[0][3][3]); SBAR(); }
  { MF_O(144, vf[4], pf[2][1]); sB[1][0][0] = exp_s(ffma_s(sB[1][0][0], csc, nmc[1])); PIN(sB[1][0][0]); SBAR(); }
  { MF_O(20, vf[5], pf[0][1]); vf[3] = VRD(0, 1, 11); sB[1][0][1] = exp_s(ffma_s(sB[1][0][1], csc, nmc[1])); PIN(sB[1][0][1]); SBAR(); }
  { MF_O(84, vf[5], pf[1][1]); sB[1][0][2] = exp_s(ffma_s(sB[1][0][2], csc, nmc[1])); PIN(sB[1][0][2]); SBAR(); }
  { MF_O(148, vf[5], pf[2][1]); sB[1][0][3] = exp_s(ffma_s(sB[1][0][3], csc, nmc[1])); PIN(sB[1][0][3]); SBAR(); }
  { MF_O(24, vf[6], pf[0][1]); vf[4] = VRD(0, 1, 12); SBAR(); }
  { MF_O(88, vf[6], pf[1][1]); sB[1][1][0] = exp_s(ffma_s(sB[1][1][0], csc, nmc[1])); PIN(sB[1][1][0]); SBAR(); }
  { MF_O(152, vf[6], pf[2][1]); sB[1][1][1] = exp_s(ffma_s(sB[1][1][1], csc, nmc[1])); PIN(sB[1][1][1]); SBAR(); }
  { MF_O(28, vf[7], pf[0][1]); vf[5] = VRD(0, 1, 13); SBAR(); }
  { MF_O(92, vf[7], pf[1][1]); sB[1][1][2] = exp_s(ffma_s(sB[1][1][2], csc, nmc[1])); PIN(sB[1][1][2]); SBAR(); }
  { MF_O(156, vf[7], pf[2][1]); sB[1][1][3] = exp_s(ffma_s(sB[1][1][3], csc, nmc[1])); PIN(sB[1][1][3]); SBAR(); }
  { MF_O(32, vf[0], pf[0][1]); vf[6] = VRD(0, 1, 14); SBAR(); }
  { MF_O(96, vf[0], pf[1][1]); sB[1][2][0] = exp_s(ffma_s(sB[1][2][0], csc, nmc[1])); PIN(sB[1][2][0]); SBAR(); }
  { MF_O(160, vf[0], pf[2][1]); sB[1][2][1] = exp_s(ffma_s(sB[1][2][1], csc, nmc[1])); PIN(sB[1][2][1]); SBAR(); }
  { MF_O(36, vf[1], pf[0][1]); vf[7] = VRD(0, 1, 15); SBAR(); }
  { MF_O(100, vf[1], pf[1][1]); sB[1][2][2] = exp_s(ffma_s(sB[1][2][2], csc, nmc[1])); PIN(sB[1][2][2]); SBAR(); }
  { MF_O(164, vf[1], pf[2][1]); sB[1][2][3] = exp_s(ffma_s(sB[1][2][3], csc, nmc[1])); PIN(sB[1][2][3]); SBAR(); }
  { MF_O(40, vf[2], pf[0][1]);  SBAR(); }
  { MF_O(104, vf[2], pf[1][1]); sB[1][3][0] = exp_s(ffma_s(sB[1][3][0], csc, nmc[1])); PIN(sB[1][3][0]); SBAR(); }
  { MF_O(168, vf[2], pf[2][1]); sB[1][3][1] = exp_s(ffma_s(sB[1][3][1], csc, nmc[1])); PIN(sB[1][3][1]); SBAR(); }
  { MF_O(44, vf[3], pf[0][1]);  SBAR(); }
  { MF_O(108, vf[3], pf[1][1]); sB[1][3][2] = exp_s(ffma_s(sB[1][3][2], csc, nmc[1])); PIN(sB[1][3][2]); SBAR(); }
  { MF_O(172, vf[3], pf[2][1]); sB[1][3][3] = exp_s(ffma_s(sB[1][3][3], csc, nmc[1])); PIN(sB[1][3][3]); SBAR(); }
  { MF_O(48, vf[4], pf[0][1]);  SBAR(); }
  { MF_O(112, vf[4], pf[1][1]); sB[2][0][0] = exp_s(ffma_s(sB[2][0][0], csc, nmc[2])); PIN(sB[2][0][0]); SBAR(); }
  { MF_O(176, vf[4], pf[2][1]); sB[2][0][1] = exp_s(ffma_s(sB[2][0][1], csc, nmc[2])); PIN(sB[2][0][1]); SBAR(); }
  { MF_O(52, vf[5], pf[0][1]);  SBAR(); }
  { MF_O(116, vf[5], pf[1][1]); sB[2][0][2] = exp_s(ffma_s(sB[2][0][2], csc, nmc[2])); PIN(sB[2][0][2]); SBAR(); }
  { MF_O(180, vf[5], pf[2][1]); sB[2][0][3] = exp_s(ffma_s(sB[2][0][3], csc, nmc[2])); PIN(sB[2][0][3]); SBAR(); }
  { MF_O(56, vf[6], pf[0][1]);  SBAR(); }
  { MF_O(120, vf[6], pf[1][1]); sB[2][1][0] = exp_s(ffma_s(sB[2][1][0], csc, nmc[2])); PIN(sB[2][1][0]); SBAR(); }
  { MF_O(184, vf[6], pf[2][1]); sB[2][1][1] = exp_s(ffma_s(sB[2][1][1], csc, nmc[2])); PIN(sB[2][1][1]); SBAR(); }
  { MF_O(60, vf[7], pf[0][1]);  SBAR(); }
  { MF_O(124, vf[7], pf[1][1]); sB[2][1][2] = exp_s(ffma_s(sB[2][1][2], csc, nmc[2])); PIN(sB[2][1][2]); SBAR(); }
  { MF_O(188, vf[7], pf[2][1]); sB[2][1][3] = exp_s(ffma_s(sB[2][1][3], csc, nmc[2])); PIN(sB[2][1][3]); SBAR(); }
  if (__builtin_expect(pend, 0)) {
    asm volatile("s_nop 7\n\ts_nop 7" ::: "memory");
      { float t0, t1; asm volatile("v_accvgpr_read_b32 %0, a0\n\tv_mul_f32 %0, %2, %0\n\tv_accvgpr_write_b32 a0, %0\n\t" "v_accvgpr_read_b32 %1, a1\n\tv_mul_f32 %1, %2, %1\n\tv_accvgpr_write_b32 a1, %1\n\t" "v_accvgpr_read_b32 %0, a2\n\tv_mul_f32 %0, %2, %0\n\tv_accvgpr_write_b32 a2, %0\n\t" "v_accvgpr_read_b32 %1, a3\n\tv_mul_f32 %1, %2, %1\n\tv_accvgpr_write_b32 a3, %1\n\t" "v_accvgpr_read_b32 %0, a4\n\tv_mul_f32 %0, %2, %0\n\tv_accvgpr_write_b32 a4, %0\n\t" "v_accvgpr_read_b32 %1, a5\n\tv_mul_f32 %1, %2, %1\n\tv_accvgpr_write_b32 a5, %1\n\t" "v_accvgpr_read_b32 %0, a6\n\tv_mul_f32 %0, %2, %0\n\tv_accvgpr_write_b32 a6, %0\n\t" "v_accvgpr_read_b32 %1, a7\n\tv_mul_f32 %1, %2, %1\n\tv_accvgpr_write_b32 a7, %1\n\t" "v_accvgpr_read_b32 %0, a8\n\tv_mul_f32 %0, %2, %0\n\tv_accvgpr_write_b32 a8, %0\n\t" "v_accvgpr_read_b32 %1, a9\n\tv_mul_f32 %1, %2, %1\n\tv_accvgpr_write_b32 a9, %1\n\t" "v_accvgpr_read_b32 %0, a10\n\tv_mul_f32 %0, %2, %0\n\tv_accvgpr_write_b32 a10, %0\n\t" "v_accvgpr_read_b32 %1, a11\n\tv_mul_f32 %1, %2, %1\n\tv_accvgpr_write_b32 a11, %1\n\t" "v_accvgpr_read_b32 %0, a12\n\tv_mul_f32 %0, %2, %0\n\tv_accvgpr_write_b32 a12, %0\n\t" "v_accvgpr_read_b32 %1, a13\n\tv_mul_f32 %1, %2, %1\n\tv_accvgpr_write_b32 a13, %1\n\t" "v_accvgpr_read_b32 %0, a14\n\tv_mul_f32 %0, %2, %0\n\tv_accvgpr_write_b32 a14, %0\n\t" "v_accvgpr_read_b32 %1, a15\n\tv_mul_f32 %1, %2, %1\n\tv_accvgpr_write_b32 a15, %1\n\t" "v_accvgpr_read_b32 %0, a16\n\tv_mul_f32 %0, %2, %0\n\tv_accvgpr_write_b32 a16, %0\n\t" "v_accvgpr_read_b32 %1, a17\n\tv_mul_f32 %1, %2, %1\n\tv_accvgpr_write_b32 a17, %1\n\t" "v_accvgpr_read_b32 %0, a18\n\tv_mul_f32 %0, %2, %0\n\tv_accvgpr_write_b32 a18, %0\n\t" "v_accvgpr_read_b32 %1, a19\n\tv_mul_f32 %1, %2, %1\n\tv_accvgpr_write_b32 a19, %1\n\t" "v_accvgpr_read_b32 %0, a20\n\tv_mul_f32 %0, %2, %0\n\tv_accvgpr_write_b32 a20, %0\n\t" "v_accvgpr_read_b32 %1, a21\n\tv_mul_f32 %1, %2, %1\n\tv_accvgpr_write_b32 a21, %1\n\t" "v_accvgpr_read_b32 %0, a22\n\tv_mul_f32 %0, %2, %0\n\tv_accvgpr_write_b32 a22, %0\n\t" "v_accvgpr_read_b32 %1, a23\n\tv_mul_f32 %1, %2, %1\n\tv_accvgpr_write_b32 a23, %1\n\t" "v_accvgpr_read_b32 %0, a24\n\tv_mul_f32 %0, %2, %0\n\tv_accvgpr_write_b32 a24, %0\n\t" "v_accvgpr_read_b32 %1, a25\n\tv_mul_f32 %1, %2, %1\n\tv_accvgpr_write_b32 a25, %1\n\t" "v_accvgpr_read_b32 %0, a26\n\tv_mul_f32 %0, %2, %0\n\tv_accvgpr_write_b32 a26, %0\n\t" "v_accvgpr_read_b32 %1, a27\n\tv_mul_f32 %1, %2, %1\n\tv_accvgpr_write_b32 a27, %1\n\t" "v_accvgpr_read_b32 %0, a28\n\tv_mul_f32 %0, %2, %0\n\tv_accvgpr_write_b32 a28, %0\n\t" "v_accvgpr_read_b32 %1, a29\n\tv_mul_f32 %1, %2, %1\n\tv_accvgpr_write_b32 a29, %1\n\t" "v_accvgpr_read_b32 %0, a30\n\tv_mul_f32 %0, %2, %0\n\tv_accvgpr_write_b32 a30, %0\n\t" "v_accvgpr_read_b32 %1, a31\n\tv_mul_f32 %1, %2, %1\n\tv_accvgpr_write_b32 a31, %1\n\t" "v_accvgpr_read_b32 %0, a32\n\tv_mul_f32 %0, %2, %0\n\tv_accvgpr_write_b32 a32, %0\n\t" "v_accvgpr_read_b32 %1, a33\n\tv_mul_f32 %1, %2, %1\n\tv_accvgpr_write_b32 a33, %1\n\t" "v_accvgpr_read_b32 %0, a34\n\tv_mul_f32 %0, %2, %0\n\tv_accvgpr_write_b32 a34, %0\n\t" "v_accvgpr_read_b32 %1, a35\n\tv_mul_f32 %1, %2, %1\n\tv_accvgpr_write_b32 a35, %1\n\t" "v_accvgpr_read_b32 %0, a36\n\tv_mul_f32 %0, %2, %0\n\tv_accvgpr_write_b32 a36, %0\n\t" "v_accvgpr_read_b32 %1, a37\n\tv_mul_f32 %1, %2, %1\n\tv_accvgpr_write_b32 a37, %1\n\t" "v_accvgpr_read_b32 %0, a38\n\tv_mul_f32 %0, %2, %0\n\tv_accvgpr_write_b32 a38, %0\n\t" "v_accvgpr_read_b32 %1, a39\n\tv_mul_f32 %1, %2, %1\n\tv_accvgpr_write_b32 a39, %1\n\t" "v_accvgpr_read_b32 %0, a40\n\tv_mul_f32 %0, %2, %0\n\tv_accvgpr_write_b32 a40, %0\n\t" "v_accvgpr_read_b32 %1, a41\n\tv_mul_f32 %1, %2, %1\n\tv_accvgpr_write_b32 a41, %1\n\t" "v_accvgpr_read_b32 %0, a42\n\tv_mul_f32 %0, %2, %0\n\tv_accvgpr_write_b32 a42, %0\n\t" "v_accvgpr_read_b32 %1, a43\n\tv_mul_f32 %1, %2, %1\n\tv_accvgpr_write_b32 a43, %1\n\t" "v_accvgpr_read_b32 %0, a44\n\tv_mul_f32 %0, %2, %0\n\tv_accvgpr_write_b32 a44, %0\n\t" "v_accvgpr_read_b32 %1, a45\n\tv_mul_f32 %1, %2, %1\n\tv_accvgpr_write_b32 a45, %1\n\t" "v_accvgpr_read_b32 %0, a46\n\tv_mul_f32 %0, %2, %0\n\tv_accvgpr_write_b32 a46, %0\n\t" "v_accvgpr_read_b32 %1, a47\n\tv_mul_f32 %1, %2, %1\n\tv_accvgpr_write_b32 a47, %1\n\t" "v_accvgpr_read_b32 %0, a48\n\tv_mul_f32 %0, %2, %0\n\tv_accvgpr_write_b32 a48, %0\n\t" "v_accvgpr_read_b32 %1, a49\n\tv_mul_f32 %1, %2, %1\n\tv_accvgpr_write_b32 a49, %1\n\t" "v_accvgpr_read_b32 %0, a50\n\tv_mul_f32 %0, %2, %0\n\tv_accvgpr_write_b32 a50, %0\n\t" "v_accvgpr_read_b32 %1, a51\n\tv_mul_f32 %1, %2, %1\n\tv_accvgpr_write_b32 a51, %1\n\t" "v_accvgpr_read_b32 %0, a52\n\tv_mul_f32 %0, %2, %0\n\tv_accvgpr_write_b32 a52, %0\n\t" "v_accvgpr_read_b32 %1, a53\n\tv_mul_f32 %1, %2, %1\n\tv_accvgpr_write_b32 a53, %1\n\t" "v_accvgpr_read_b32 %0, a54\n\tv_mul_f32 %0, %2, %0\n\tv_accvgpr_write_b32 a54, %0\n\t" "v_accvgpr_read_b32 %1, a55\n\tv_mul_f32 %1, %2, %1\n\tv_accvgpr_write_b32 a55, %1\n\t" "v_accvgpr_read_b32 %0, a56\n\tv_mul_f32 %0, %2, %0\n\tv_accvgpr_write_b32 a56, %0\n\t" "v_accvgpr_read_b32 %1, a57\n\tv_mul_f32 %1, %2, %1\n\tv_accvgpr_write_b32 a57, %1\n\t" "v_accvgpr_read_b32 %0, a58\n\tv_mul_f32 %0, %2, %0\n\tv_accvgpr_write_b32 a58, %0\n\t" "v_accvgpr_read_b32 %1, a59\n\tv_mul_f32 %1, %2, %1\n\tv_accvgpr_write_b32 a59, %1\n\t" "v_accvgpr_read_b32 %0, a60\n\tv_mul_f32 %0, %2, %0\n\tv_accvgpr_write_b32 a60, %0\n\t" "v_accvgpr_read_b32 %1, a61\n\tv_mul_f32 %1, %2, %1\n\tv_accvgpr_write_b32 a61, %1\n\t" "v_accvgpr_read_b32 %0, a62\n\tv_mul_f32 %0, %2, %0\n\tv_accvgpr_write_b32 a62, %0\n\t" "v_accvgpr_read_b32 %1, a63\n\tv_mul_f32 %1, %2, %1\n\tv_accvgpr_write_b32 a63, %1\n\t" : "=&v"(t0), "=&v"(t1) : "v"(alv[0]) : ACLOB); }
      { float t0, t1; asm volatile("v_accvgpr_read_b32 %0, a64\n\tv_mul_f32 %0, %2, %0\n\tv_accvgpr_write_b32 a64, %0\n\t" "v_accvgpr_read_b32 %1, a65\n\tv_mul_f32 %1, %2, %1\n\tv_accvgpr_write_b32 a65, %1\n\t" "v_accvgpr_read_b32 %0, a66\n\tv_mul_f32 %0, %2, %0\n\tv_accvgpr_write_b32 a66, %0\n\t" "v_accvgpr_read_b32 %1, a67\n\tv_mul_f32 %1, %2, %1\n\tv_accvgpr_write_b32 a67, %1\n\t" "v_accvgpr_read_b32 %0, a68\n\tv_mul_f32 %0, %2, %0\n\tv_accvgpr_write_b32 a68, %0\n\t" "v_accvgpr_read_b32 %1, a69\n\tv_mul_f32 %1, %2, %1\n\tv_accvgpr_write_b32 a69, %1\n\t" "v_accvgpr_read_b32 %0, a70\n\tv_mul_f32 %0, %2, %0\n\tv_accvgpr_write_b32 a70, %0\n\t" "v_accvgpr_read_b32 %1, a71\n\tv_mul_f32 %1, %2, %1\n\tv_accvgpr_write_b32 a71, %1\n\t" "v_accvgpr_read_b32 %0, a72\n\tv_mul_f32 %0, %2, %0\n\tv_accvgpr_write_b32 a72, %0\n\t" "v_accvgpr_read_b32 %1, a73\n\tv_mul_f32 %1, %2, %1\n\tv_accvgpr_write_b32 a73, %1\n\t" "v_accvgpr_read_b32 %0, a74\n\tv_mul_f32 %0, %2, %0\n\tv_accvgpr_write_b32 a74, %0\n\t" "v_accvgpr_read_b32 %1, a75\n\tv_mul_f32 %1, %2, %1\n\tv_accvgpr_write_b32 a75, %1\n\t" "v_accvgpr_read_b32 %0, a76\n\tv_mul_f32 %0, %2, %0\n\tv_accvgpr_write_b32 a76, %0\n\t" "v_accvgpr_read_b32 %1, a77\n\tv_mul_f32 %1, %2, %1\n\tv_accvgpr_write_b32 a77, %1\n\t" "v_accvgpr_read_b32 %0, a78\n\tv_mul_f32 %0, %2, %0\n\tv_accvgpr_write_b32 a78, %0\n\t" "v_accvgpr_read_b32 %1, a79\n\tv_mul_f32 %1, %2, %1\n\tv_accvgpr_write_b32 a79, %1\n\t" "v_accvgpr_read_b32 %0, a80\n\tv_mul_f32 %0, %2, %0\n\tv_accvgpr_write_b32 a80, %0\n\t" "v_accvgpr_read_b32 %1, a81\n\tv_mul_f32 %1, %2, %1\n\tv_accvgpr_write_b32 a81, %1\n\t" "v_accvgpr_read_b32 %0, a82\n\tv_mul_f32 %0, %2, %0\n\tv_accvgpr_write_b32 a82, %0\n\t" "v_accvgpr_read_b32 %1, a83\n\tv_mul_f32 %1, %2, %1\n\tv_accvgpr_write_b32 a83, %1\n\t" "v_accvgpr_read_b32 %0, a84\n\tv_mul_f32 %0, %2, %0\n\tv_accvgpr_write_b32 a84, %0\n\t" "v_accvgpr_read_b32 %1, a85\n\tv_mul_f32 %1, %2, %1\n\tv_accvgpr_write_b32 a85, %1\n\t" "v_accvgpr_read_b32 %0, a86\n\tv_mul_f32 %0, %2, %0\n\tv_accvgpr_write_b32 a86, %0\n\t" "v_accvgpr_read_b32 %1, a87\n\tv_mul_f32 %1, %2, %1\n\tv_accvgpr_write_b32 a87, %1\n\t" "v_accvgpr_read_b32 %0, a88\n\tv_mul_f32 %0, %2, %0\n\tv_accvgpr_write_b32 a88, %0\n\t" "v_accvgpr_read_b32 %1, a89\n\tv_mul_f32 %1, %2, %1\n\tv_accvgpr_write_b32 a89, %1\n\t" "v_accvgpr_read_b32 %0, a90\n\tv_mul_f32 %0, %2, %0\n\tv_accvgpr_write_b32 a90, %0\n\t" "v_accvgpr_read_b32 %1, a91\n\tv_mul_f32 %1, %2, %1\n\tv_accvgpr_write_b32 a91, %1\n\t" "v_accvgpr_read_b32 %0, a92\n\tv_mul_f32 %0, %2, %0\n\tv_accvgpr_write_b32 a92, %0\n\t" "v_accvgpr_read_b32 %1, a93\n\tv_mul_f32 %1, %2, %1\n\tv_accvgpr_write_b32 a93, %1\n\t" "v_accvgpr_read_b32 %0, a94\n\tv_mul_f32 %0, %2, %0\n\tv_accvgpr_write_b32 a94, %0\n\t" "v_accvgpr_read_b32 %1, a95\n\tv_mul_f32 %1, %2, %1\n\tv_accvgpr_write_b32 a95, %1\n\t" "v_accvgpr_read_b32 %0, a96\n\tv_mul_f32 %0, %2, %0\n\tv_accvgpr_write_b32 a96, %0\n\t" "v_accvgpr_read_b32 %1, a97\n\tv_mul_f32 %1, %2, %1\n\tv_accvgpr_write_b32 a97, %1\n\t" "v_accvgpr_read_b32 %0, a98\n\tv_mul_f32 %0, %2, %0\n\tv_accvgpr_write_b32 a98, %0\n\t" "v_accvgpr_read_b32 %1, a99\n\tv_mul_f32 %1, %2, %1\n\tv_accvgpr_write_b32 a99, %1\n\t" "v_accvgpr_read_b32 %0, a100\n\tv_mul_f32 %0, %2, %0\n\tv_accvgpr_write_b32 a100, %0\n\t" "v_accvgpr_read_b32 %1, a101\n\tv_mul_f32 %1, %2, %1\n\tv_accvgpr_write_b32 a101, %1\n\t" "v_accvgpr_read_b32 %0, a102\n\tv_mul_f32 %0, %2, %0\n\tv_accvgpr_write_b32 a102, %0\n\t" "v_accvgpr_read_b32 %1, a103\n\tv_mul_f32 %1, %2, %1\n\tv_accvgpr_write_b32 a103, %1\n\t" "v_accvgpr_read_b32 %0, a104\n\tv_mul_f32 %0, %2, %0\n\tv_accvgpr_write_b32 a104, %0\n\t" "v_accvgpr_read_b32 %1, a105\n\tv_mul_f32 %1, %2, %1\n\tv_accvgpr_write_b32 a105, %1\n\t" "v_accvgpr_read_b32 %0, a106\n\tv_mul_f32 %0, %2, %0\n\tv_accvgpr_write_b32 a106, %0\n\t" "v_accvgpr_read_b32 %1, a107\n\tv_mul_f32 %1, %2, %1\n\tv_accvgpr_write_b32 a107, %1\n\t" "v_accvgpr_read_b32 %0, a108\n\tv_mul_f32 %0, %2, %0\n\tv_accvgpr_write_b32 a108, %0\n\t" "v_accvgpr_read_b32 %1, a109\n\tv_mul_f32 %1, %2, %1\n\tv_accvgpr_write_b32 a109, %1\n\t" "v_accvgpr_read_b32 %0, a110\n\tv_mul_f32 %0, %2, %0\n\tv_accvgpr_write_b32 a110, %0\n\t" "v_accvgpr_read_b32 %1, a111\n\tv_mul_f32 %1, %2, %1\n\tv_accvgpr_write_b32 a111, %1\n\t" "v_accvgpr_read_b32 %0, a112\n\tv_mul_f32 %0, %2, %0\n\tv_accvgpr_write_b32 a112, %0\n\t" "v_accvgpr_read_b32 %1, a113\n\tv_mul_f32 %1, %2, %1\n\tv_accvgpr_write_b32 a113, %1\n\t" "v_accvgpr_read_b32 %0, a114\n\tv_mul_f32 %0, %2, %0\n\tv_accvgpr_write_b32 a114, %0\n\t" "v_accvgpr_read_b32 %1, a115\n\tv_mul_f32 %1, %2, %1\n\tv_accvgpr_write_b32 a115, %1\n\t" "v_accvgpr_read_b32 %0, a116\n\tv_mul_f32 %0, %2, %0\n\tv_accvgpr_write_b32 a116, %0\n\t" "v_accvgpr_read_b32 %1, a117\n\tv_mul_f32 %1, %2, %1\n\tv_accvgpr_write_b32 a117, %1\n\t" "v_accvgpr_read_b32 %0, a118\n\tv_mul_f32 %0, %2, %0\n\tv_accvgpr_write_b32 a118, %0\n\t" "v_accvgpr_read_b32 %1, a119\n\tv_mul_f32 %1, %2, %1\n\tv_accvgpr_write_b32 a119, %1\n\t" "v_accvgpr_read_b32 %0, a120\n\tv_mul_f32 %0, %2, %0\n\tv_accvgpr_write_b32 a120, %0\n\t" "v_accvgpr_read_b32 %1, a121\n\tv_mul_f32 %1, %2, %1\n\tv_accvgpr_write_b32 a121, %1\n\t" "v_accvgpr_read_b32 %0, a122\n\tv_mul_f32 %0, %2, %0\n\tv_accvgpr_write_b32 a122, %0\n\t" "v_accvgpr_read_b32 %1, a123\n\tv_mul_f32 %1, %2, %1\n\tv_accvgpr_write_b32 a123, %1\n\t" "v_accvgpr_read_b32 %0, a124\n\tv_mul_f32 %0, %2, %0\n\tv_accvgpr_write_b32 a124, %0\n\t" "v_accvgpr_read_b32 %1, a125\n\tv_mul_f32 %1, %2, %1\n\tv_accvgpr_write_b32 a125, %1\n\t" "v_accvgpr_read_b32 %0, a126\n\tv_mul_f32 %0, %2, %0\n\tv_accvgpr_write_b32 a126, %0\n\t" "v_accvgpr_read_b32 %1, a127\n\tv_mul_f32 %1, %2, %1\n\tv_accvgpr_write_b32 a127, %1\n\t" : "=&v"(t0), "=&v"(t1) : "v"(alv[1]) : ACLOB); }
      { float t0, t1; asm volatile("v_accvgpr_read_b32 %0, a128\n\tv_mul_f32 %0, %2, %0\n\tv_accvgpr_write_b32 a128, %0\n\t" "v_accvgpr_read_b32 %1, a129\n\tv_mul_f32 %1, %2, %1\n\tv_accvgpr_write_b32 a129, %1\n\t" "v_accvgpr_read_b32 %0, a130\n\tv_mul_f32 %0, %2, %0\n\tv_accvgpr_write_b32 a130, %0\n\t" "v_accvgpr_read_b32 %1, a131\n\tv_mul_f32 %1, %2, %1\n\tv_accvgpr_write_b32 a131, %1\n\t" "v_accvgpr_read_b32 %0, a132\n\tv_mul_f32 %0, %2, %0\n\tv_accvgpr_write_b32 a132, %0\n\t" "v_accvgpr_read_b32 %1, a133\n\tv_mul_f32 %1, %2, %1\n\tv_accvgpr_write_b32 a133, %1\n\t" "v_accvgpr_read_b32 %0, a134\n\tv_mul_f32 %0, %2, %0\n\tv_accvgpr_write_b32 a134, %0\n\t" "v_accvgpr_read_b32 %1, a135\n\tv_mul_f32 %1, %2, %1\n\tv_accvgpr_write_b32 a135, %1\n\t" "v_accvgpr_read_b32 %0, a136\n\tv_mul_f32 %0, %2, %0\n\tv_accvgpr_write_b32 a136, %0\n\t" "v_accvgpr_read_b32 %1, a137\n\tv_mul_f32 %1, %2, %1\n\tv_accvgpr_write_b32 a137, %1\n\t" "v_accvgpr_read_b32 %0, a138\n\tv_mul_f32 %0, %2, %0\n\tv_accvgpr_write_b32 a138, %0\n\t" "v_accvgpr_read_b32 %1, a139\n\tv_mul_f32 %1, %2, %1\n\tv_accvgpr_write_b32 a139, %1\n\t" "v_accvgpr_read_b32 %0, a140\n\tv_mul_f32 %0, %2, %0\n\tv_accvgpr_write_b32 a140, %0\n\t" "v_accvgpr_read_b32 %1, a141\n\tv_mul_f32 %1, %2, %1\n\tv_accvgpr_write_b32 a141, %1\n\t" "v_accvgpr_read_b32 %0, a142\n\tv_mul_f32 %0, %2, %0\n\tv_accvgpr_write_b32 a142, %0\n\t" "v_accvgpr_read_b32 %1, a143\n\tv_mul_f32 %1, %2, %1\n\tv_accvgpr_write_b32 a143, %1\n\t" "v_accvgpr_read_b32 %0, a144\n\tv_mul_f32 %0, %2, %0\n\tv_accvgpr_write_b32 a144, %0\n\t" "v_accvgpr_read_b32 %1, a145\n\tv_mul_f32 %1, %2, %1\n\tv_accvgpr_write_b32 a145, %1\n\t" "v_accvgpr_read_b32 %0, a146\n\tv_mul_f32 %0, %2, %0\n\tv_accvgpr_write_b32 a146, %0\n\t" "v_accvgpr_read_b32 %1, a147\n\tv_mul_f32 %1, %2, %1\n\tv_accvgpr_write_b32 a147, %1\n\t" "v_accvgpr_read_b32 %0, a148\n\tv_mul_f32 %0, %2, %0\n\tv_accvgpr_write_b32 a148, %0\n\t" "v_accvgpr_read_b32 %1, a149\n\tv_mul_f32 %1, %2, %1\n\tv_accvgpr_write_b32 a149, %1\n\t" "v_accvgpr_read_b32 %0, a150\n\tv_mul_f32 %0, %2, %0\n\tv_accvgpr_write_b32 a150, %0\n\t" "v_accvgpr_read_b32 %1, a151\n\tv_mul_f32 %1, %2, %1\n\tv_accvgpr_write_b32 a151, %1\n\t" "v_accvgpr_read_b32 %0, a152\n\tv_mul_f32 %0, %2, %0\n\tv_accvgpr_write_b32 a152, %0\n\t" "v_accvgpr_read_b32 %1, a153\n\tv_mul_f32 %1, %2, %1\n\tv_accvgpr_write_b32 a153, %1\n\t" "v_accvgpr_read_b32 %0, a154\n\tv_mul_f32 %0, %2, %0\n\tv_accvgpr_write_b32 a154, %0\n\t" "v_accvgpr_read_b32 %1, a155\n\tv_mul_f32 %1, %2, %1\n\tv_accvgpr_write_b32 a155, %1\n\t" "v_accvgpr_read_b32 %0, a156\n\tv_mul_f32 %0, %2, %0\n\tv_accvgpr_write_b32 a156, %0\n\t" "v_accvgpr_read_b32 %1, a157\n\tv_mul_f32 %1, %2, %1\n\tv_accvgpr_write_b32 a157, %1\n\t" "v_accvgpr_read_b32 %0, a158\n\tv_mul_f32 %0, %2, %0\n\tv_accvgpr_write_b32 a158, %0\n\t" "v_accvgpr_read_b32 %1, a159\n\tv_mul_f32 %1, %2, %1\n\tv_accvgpr_write_b32 a159, %1\n\t" "v_accvgpr_read_b32 %0, a160\n\tv_mul_f32 %0, %2, %0\n\tv_accvgpr_write_b32 a160, %0\n\t" "v_accvgpr_read_b32 %1, a161\n\tv_mul_f32 %1, %2, %1\n\tv_accvgpr_write_b32 a161, %1\n\t" "v_accvgpr_read_b32 %0, a162\n\tv_mul_f32 %0, %2, %0\n\tv_accvgpr_write_b32 a162, %0\n\t" "v_accvgpr_read_b32 %1, a163\n\tv_mul_f32 %1, %2, %1\n\tv_accvgpr_write_b32 a163, %1\n\t" "v_accvgpr_read_b32 %0, a164\n\tv_mul_f32 %0, %2, %0\n\tv_accvgpr_write_b32 a164, %0\n\t" "v_accvgpr_read_b32 %1, a165\n\tv_mul_f32 %1, %2, %1\n\tv_accvgpr_write_b32 a165, %1\n\t" "v_accvgpr_read_b32 %0, a166\n\tv_mul_f32 %0, %2, %0\n\tv_accvgpr_write_b32 a166, %0\n\t" "v_accvgpr_read_b32 %1, a167\n\tv_mul_f32 %1, %2, %1\n\tv_accvgpr_write_b32 a167, %1\n\t" "v_accvgpr_read_b32 %0, a168\n\tv_mul_f32 %0, %2, %0\n\tv_accvgpr_write_b32 a168, %0\n\t" "v_accvgpr_read_b32 %1, a169\n\tv_mul_f32 %1, %2, %1\n\tv_accvgpr_write_b32 a169, %1\n\t" "v_accvgpr_read_b32 %0, a170\n\tv_mul_f32 %0, %2, %0\n\tv_accvgpr_write_b32 a170, %0\n\t" "v_accvgpr_read_b32 %1, a171\n\tv_mul_f32 %1, %2, %1\n\tv_accvgpr_write_b32 a171, %1\n\t" "v_accvgpr_read_b32 %0, a172\n\tv_mul_f32 %0, %2, %0\n\tv_accvgpr_write_b32 a172, %0\n\t" "v_accvgpr_read_b32 %1, a173\n\tv_mul_f32 %1, %2, %1\n\tv_accvgpr_write_b32 a173, %1\n\t" "v_accvgpr_read_b32 %0, a174\n\tv_mul_f32 %0, %2, %0\n\tv_accvgpr_write_b32 a174, %0\n\t" "v_accvgpr_read_b32 %1, a175\n\tv_mul_f32 %1, %2, %1\n\tv_accvgpr_write_b32 a175, %1\n\t" "v_accvgpr_read_b32 %0, a176\n\tv_mul_f32 %0, %2, %0\n\tv_accvgpr_write_b32 a176, %0\n\t" "v_accvgpr_read_b32 %1, a177\n\tv_mul_f32 %1, %2, %1\n\tv_accvgpr_write_b32 a177, %1\n\t" "v_accvgpr_read_b32 %0, a178\n\tv_mul_f32 %0, %2, %0\n\tv_accvgpr_write_b32 a178, %0\n\t" "v_accvgpr_read_b32 %1, a179\n\tv_mul_f32 %1, %2, %1\n\tv_accvgpr_write_b32 a179, %1\n\t" "v_accvgpr_read_b32 %0, a180\n\tv_mul_f32 %0, %2, %0\n\tv_accvgpr_write_b32 a180, %0\n\t" "v_accvgpr_read_b32 %1, a181\n\tv_mul_f32 %1, %2, %1\n\tv_accvgpr_write_b32 a181, %1\n\t" "v_accvgpr_read_b32 %0, a182\n\tv_mul_f32 %0, %2, %0\n\tv_accvgpr_write_b32 a182, %0\n\t" "v_accvgpr_read_b32 %1, a183\n\tv_mul_f32 %1, %2, %1\n\tv_accvgpr_write_b32 a183, %1\n\t" "v_accvgpr_read_b32 %0, a184\n\tv_mul_f32 %0, %2, %0\n\tv_accvgpr_write_b32 a184, %0\n\t" "v_accvgpr_read_b32 %1, a185\n\tv_mul_f32 %1, %2, %1\n\tv_accvgpr_write_b32 a185, %1\n\t" "v_accvgpr_read_b32 %0, a186\n\tv_mul_f32 %0, %2, %0\n\tv_accvgpr_write_b32 a186, %0\n\t" "v_accvgpr_read_b32 %1, a187\n\tv_mul_f32 %1, %2, %1\n\tv_accvgpr_write_b32 a187, %1\n\t" "v_accvgpr_read_b32 %0, a188\n\tv_mul_f32 %0, %2, %0\n\tv_accvgpr_write_b32 a188, %0\n\t" "v_accvgpr_read_b32 %1, a189\n\tv_mul_f32 %1, %2, %1\n\tv_accvgpr_write_b32 a189, %1\n\t" "v_accvgpr_read_b32 %0, a190\n\tv_mul_f32 %0, %2, %0\n\tv_accvgpr_write_b32 a190, %0\n\t" "v_accvgpr_read_b32 %1, a191\n\tv_mul_f32 %1, %2, %1\n\tv_accvgpr_write_b32 a191, %1\n\t" : "=&v"(t0), "=&v"(t1) : "v"(alv[2]) : ACLOB); }
    asm volatile("s_nop 1" ::: "memory"); pend = 0;
  }
  SBAR();
  asm volatile("s_waitcnt vmcnt(16)\n\ts_barrier" ::: "memory");
  kf[0] = KRD(0, 0, 0); kf[1] = KRD(0, 0, 1); kf[2] = KRD(0, 0, 2); kf[3] = KRD(0, 0, 3); kf[4] = KRD(0, 0, 4); kf[5] = KRD(0, 0, 5);
  { MF_S0A(sA[0][0], kf[0], 192); kf[6] = KRD(0, 0, 6); SBAR(); }
  { MF_S0A(sA[1][0], kf[0], 224); DMA_V1(t + 2, 0, 0); SBAR(); }
  { MF_S0V(sA[2][0], kf[0], qf2[0]); sB[2][2][0] = exp_s(ffma_s(sB[2][2][0], csc, nmc[2])); PIN(sB[2][2][0]); SBAR(); }
  { MF_SA(sA[0][0], kf[1], 196); kf[7] = KRD(0, 0, 7); SBAR(); }
  { MF_SA(sA[1][0], kf[1], 228); DMA_V1(t + 2, 0, 1); SBAR(); }
  { MF_SV(sA[2][0], kf[1], qf2[1]); sB[2][2][1] = exp_s(ffma_s(sB[2][2][1], csc, nmc[2])); PIN(sB[2][2][1]); SBAR(); }
  { MF_SA(sA[0][0], kf[2], 200); kf[0] = KRD(0, 1, 0); SBAR(); }
  { MF_SA(sA[1][0], kf[2], 232); DMA_V1(t + 2, 0, 2); SBAR(); }
  { MF_SV(sA[2][0], kf[2], qf2[2]); sB[2][2][2] = exp_s(ffma_s(sB[2][2][2], csc, nmc[2])); PIN(sB[2][2][2]); SBAR(); }
  { MF_SA(sA[0][0], kf[3], 204); kf[1] = KRD(0, 1, 1); SBAR(); }
  { MF_SA(sA[1][0], kf[3], 236); DMA_V1(t + 2, 0, 3); SBAR(); }
  { MF_SV(sA[2][0], kf[3], qf2[3]); sB[2][2][3] = exp_s(ffma_s(sB[2][2][3], csc, nmc[2])); PIN(sB[2][2][3]); SBAR(); }
  { MF_SA(sA[0][0], kf[4], 208); kf[2] = KRD(0, 1, 2); SBAR(); }
  { MF_SA(sA[1][0], kf[4], 240); DMA_V1(t + 2, 0, 4); SBAR(); }
  { MF_SV(sA[2][0], kf[4], qf2[4]); sB[2][3][0] = exp_s(ffma_s(sB[2][3][0], csc, nmc[2])); PIN(sB[2][3][0]); SBAR(); }
  { MF_SA(sA[0][0], kf[5], 212); kf[3] = KRD(0, 1, 3); SBAR(); }
  { MF_SA(sA[1][0], kf[5], 244); DMA_V1(t + 2, 0, 5); SBAR(); }
  { MF_SV(sA[2][0], kf[5], qf2[5]); sB[2][3][1] = exp_s(ffma_s(sB[2][3][1], csc, nmc[2])); PIN(sB[2][3][1]); SBAR(); }
  { MF_SA(sA[0][0], kf[6], 216); kf[4] = KRD(0, 1, 4); SBAR(); }
  { MF_SA(sA[1][0], kf[6], 248); DMA_V1(t + 2, 0, 6); SBAR(); }
  { MF_SV(sA[2][0], kf[6], qf2[6]); sB[2][3][2] = exp_s(ffma_s(sB[2][3][2], csc, nmc[2])); PIN(sB[2][3][2]); SBAR(); }
  { MF_SA(sA[0][0], kf[7], 220); kf[5] = KRD(0, 1, 5); SBAR(); }
  { MF_SA(sA[1][0], kf[7], 252); DMA_V1(t + 2, 0, 7); SBAR(); }
  { MF_SV(sA[2][0], kf[7], qf2[7]); sB[2][3][3] = exp_s(ffma_s(sB[2][3][3], csc, nmc[2])); PIN(sB[2][3][3]); SBAR(); }
  { MF_S0A(sA[0][1], kf[0], 192); kf[6] = KRD(0, 1, 6); SBAR(); }
  { MF_S0A(sA[1][1], kf[0], 224); l[0] = fadd_s(l[0], sB[0][0][0]); PIN(l[0]); l[0] = fadd_s(l[0], sB[0][0][1]); PIN(l[0]); SBAR(); }
  { MF_S0V(sA[2][1], kf[0], qf2[0]); pw[0][0][0] = cvtpk(sB[0][0][0], sB[0][0][1]); PIN(pw[0][0][0]); SBAR(); }
  { MF_SA(sA[0][1], kf[1], 196); kf[7] = KRD(0, 1, 7); SBAR(); }
  { MF_SA(sA[1][1], kf[1], 228); l[0] = fadd_s(l[0], sB[0][0][2]); PIN(l[0]); l[0] = fadd_s(l[0], sB[0][0][3]); PIN(l[0]); SBAR(); }
  { MF_SV(sA[2][1], kf[1], qf2[1]); pw[0][0][1] = cvtpk(sB[0][0][2], sB[0][0][3]); PIN(pw[0][0][1]); SBAR(); }
  { MF_SA(sA[0][1], kf[2], 200); kf[0] = KRD(0, 2, 0); SBAR(); }
  { MF_SA(sA[1][1], kf[2], 232); l[0] = fadd_s(l[0], sB[0][1][0]); PIN(l[0]); l[0] = fadd_s(l[0], sB[0][1][1]); PIN(l[0]); SBAR(); }
  { MF_SV(sA[2][1], kf[2], qf2[2]); pw[0][0][2] = cvtpk(sB[0][1][0], sB[0][1][1]); PIN(pw[0][0][2]); SBAR(); }
  { MF_SA(sA[0][1], kf[3], 204); kf[1] = KRD(0, 2, 1); SBAR(); }
  { MF_SA(sA[1][1], kf[3], 236); l[0] = fadd_s(l[0], sB[0][1][2]); PIN(l[0]); l[0] = fadd_s(l[0], sB[0][1][3]); PIN(l[0]); SBAR(); }
  { MF_SV(sA[2][1], kf[3], qf2[3]); pw[0][0][3] = cvtpk(sB[0][1][2], sB[0][1][3]); PIN(pw[0][0][3]); SBAR(); }
  { MF_SA(sA[0][1], kf[4], 208); kf[2] = KRD(0, 2, 2); SBAR(); }
  { MF_SA(sA[1][1], kf[4], 240); l[0] = fadd_s(l[0], sB[0][2][0]); PIN(l[0]); l[0] = fadd_s(l[0], sB[0][2][1]); PIN(l[0]); SBAR(); }
  { MF_SV(sA[2][1], kf[4], qf2[4]); pw[0][1][0] = cvtpk(sB[0][2][0], sB[0][2][1]); PIN(pw[0][1][0]); SBAR(); }
  { MF_SA(sA[0][1], kf[5], 212); kf[3] = KRD(0, 2, 3); SBAR(); }
  { MF_SA(sA[1][1], kf[5], 244); l[0] = fadd_s(l[0], sB[0][2][2]); PIN(l[0]); l[0] = fadd_s(l[0], sB[0][2][3]); PIN(l[0]); SBAR(); }
  { MF_SV(sA[2][1], kf[5], qf2[5]); pw[0][1][1] = cvtpk(sB[0][2][2], sB[0][2][3]); PIN(pw[0][1][1]); SBAR(); }
  { MF_SA(sA[0][1], kf[6], 216); kf[4] = KRD(0, 2, 4); SBAR(); }
  { MF_SA(sA[1][1], kf[6], 248); l[0] = fadd_s(l[0], sB[0][3][0]); PIN(l[0]); l[0] = fadd_s(l[0], sB[0][3][1]); PIN(l[0]); SBAR(); }
  { MF_SV(sA[2][1], kf[6], qf2[6]); pw[0][1][2] = cvtpk(sB[0][3][0], sB[0][3][1]); PIN(pw[0][1][2]); SBAR(); }
  { MF_SA(sA[0][1], kf[7], 220); kf[5] = KRD(0, 2, 5); SBAR(); }
  { MF_SA(sA[1][1], kf[7], 252); l[0] = fadd_s(l[0], sB[0][3][2]); PIN(l[0]); l[0] = fadd_s(l[0], sB[0][3][3]); PIN(l[0]); SBAR(); }
  { MF_SV(sA[2][1], kf[7], qf2[7]); pw[0][1][3] = cvtpk(sB[0][3][2], sB[0][3][3]); PIN(pw[0][1][3]); SBAR(); }
  { MF_S0A(sA[0][2], kf[0], 192); kf[6] = KRD(0, 2, 6); SBAR(); }
  { MF_S0A(sA[1][2], kf[0], 224); l[1] = fadd_s(l[1], sB[1][0][0]); PIN(l[1]); l[1] = fadd_s(l[1], sB[1][0][1]); PIN(l[1]); SBAR(); }
  { MF_S0V(sA[2][2], kf[0], qf2[0]); pw[1][0][0] = cvtpk(sB[1][0][0], sB[1][0][1]); PIN(pw[1][0][0]); SBAR(); }
  { MF_SA(sA[0][2], kf[1], 196); kf[7] = KRD(0, 2, 7); SBAR(); }
  { MF_SA(sA[1][2], kf[1], 228); l[1] = fadd_s(l[1], sB[1][0][2]); PIN(l[1]); l[1] = fadd_s(l[1], sB[1][0][3]); PIN(l[1]); SBAR(); }
  { MF_SV(sA[2][2], kf[1], qf2[1]); pw[1][0][1] = cvtpk(sB[1][0][2], sB[1][0][3]); PIN(pw[1][0][1]); SBAR(); }
  { MF_SA(sA[0][2], kf[2], 200); kf[0] = KRD(0, 3, 0); SBAR(); }
  { MF_SA(sA[1][2], kf[2], 232); l[1] = fadd_s(l[1], sB[1][1][0]); PIN(l[1]); l[1] = fadd_s(l[1], sB[1][1][1]); PIN(l[1]); SBAR(); }
  { MF_SV(sA[2][2], kf[2], qf2[2]); pw[1][0][2] = cvtpk(sB[1][1][0], sB[1][1][1]); PIN(pw[1][0][2]); SBAR(); }
  { MF_SA(sA[0][2], kf[3], 204); kf[1] = KRD(0, 3, 1); SBAR(); }
  { MF_SA(sA[1][2], kf[3], 236); l[1] = fadd_s(l[1], sB[1][1][2]); PIN(l[1]); l[1] = fadd_s(l[1], sB[1][1][3]); PIN(l[1]); SBAR(); }
  { MF_SV(sA[2][2], kf[3], qf2[3]); pw[1][0][3] = cvtpk(sB[1][1][2], sB[1][1][3]); PIN(pw[1][0][3]); SBAR(); }
  { MF_SA(sA[0][2], kf[4], 208); kf[2] = KRD(0, 3, 2); SBAR(); }
  { MF_SA(sA[1][2], kf[4], 240); l[1] = fadd_s(l[1], sB[1][2][0]); PIN(l[1]); l[1] = fadd_s(l[1], sB[1][2][1]); PIN(l[1]); SBAR(); }
  { MF_SV(sA[2][2], kf[4], qf2[4]); pw[1][1][0] = cvtpk(sB[1][2][0], sB[1][2][1]); PIN(pw[1][1][0]); SBAR(); }
  { MF_SA(sA[0][2], kf[5], 212); kf[3] = KRD(0, 3, 3); SBAR(); }
  { MF_SA(sA[1][2], kf[5], 244); l[1] = fadd_s(l[1], sB[1][2][2]); PIN(l[1]); l[1] = fadd_s(l[1], sB[1][2][3]); PIN(l[1]); SBAR(); }
  { MF_SV(sA[2][2], kf[5], qf2[5]); pw[1][1][1] = cvtpk(sB[1][2][2], sB[1][2][3]); PIN(pw[1][1][1]); SBAR(); }
  { MF_SA(sA[0][2], kf[6], 216); kf[4] = KRD(0, 3, 4); SBAR(); }
  { MF_SA(sA[1][2], kf[6], 248); l[1] = fadd_s(l[1], sB[1][3][0]); PIN(l[1]); l[1] = fadd_s(l[1], sB[1][3][1]); PIN(l[1]); SBAR(); }
  { MF_SV(sA[2][2], kf[6], qf2[6]); pw[1][1][2] = cvtpk(sB[1][3][0], sB[1][3][1]); PIN(pw[1][1][2]); SBAR(); }
  { MF_SA(sA[0][2], kf[7], 220); kf[5] = KRD(0, 3, 5); SBAR(); }
  { MF_SA(sA[1][2], kf[7], 252); l[1] = fadd_s(l[1], sB[1][3][2]); PIN(l[1]); l[1] = fadd_s(l[1], sB[1][3][3]); PIN(l[1]); SBAR(); }
  { MF_SV(sA[2][2], kf[7], qf2[7]); pw[1][1][3] = cvtpk(sB[1][3][2], sB[1][3][3]); PIN(pw[1][1][3]); SBAR(); }
  { MF_S0A(sA[0][3], kf[0], 192); kf[6] = KRD(0, 3, 6); SBAR(); }
  { MF_S0A(sA[1][3], kf[0], 224); l[2] = fadd_s(l[2], sB[2][0][0]); PIN(l[2]); l[2] = fadd_s(l[2], sB[2][0][1]); PIN(l[2]); SBAR(); }
  { MF_S0V(sA[2][3], kf[0], qf2[0]); pw[2][0][0] = cvtpk(sB[2][0][0], sB[2][0][1]); PIN(pw[2][0][0]); SBAR(); }
  { MF_SA(sA[0][3], kf[1], 196); kf[7] = KRD(0, 3, 7); SBAR(); }
  { MF_SA(sA[1][3], kf[1], 228); l[2] = fadd_s(l[2], sB[2][0][2]); PIN(l[2]); l[2] = fadd_s(l[2], sB[2][0][3]); PIN(l[2]); SBAR(); }
  { MF_SV(sA[2][3], kf[1], qf2[1]); pw[2][0][1] = cvtpk(sB[2][0][2], sB[2][0][3]); PIN(pw[2][0][1]); SBAR(); }
  { MF_SA(sA[0][3], kf[2], 200); vf[0] = VRD(1, 0, 0); SBAR(); }
  { MF_SA(sA[1][3], kf[2], 232); l[2] = fadd_s(l[2], sB[2][1][0]); PIN(l[2]); l[2] = fadd_s(l[2], sB[2][1][1]); PIN(l[2]); SBAR(); }
  { MF_SV(sA[2][3], kf[2], qf2[2]); pw[2][0][2] = cvtpk(sB[2][1][0], sB[2][1][1]); PIN(pw[2][0][2]); SBAR(); }
  { MF_SA(sA[0][3], kf[3], 204); vf[1] = VRD(1, 0, 1); SBAR(); }
  { MF_SA(sA[1][3], kf[3], 236); l[2] = fadd_s(l[2], sB[2][1][2]); PIN(l[2]); l[2] = fadd_s(l[2], sB[2][1][3]); PIN(l[2]); SBAR(); }
  { MF_SV(sA[2][3], kf[3], qf2[3]); pw[2][0][3] = cvtpk(sB[2][1][2], sB[2][1][3]); PIN(pw[2][0][3]); SBAR(); }
  { MF_SA(sA[0][3], kf[4], 208); vf[2] = VRD(1, 0, 2); SBAR(); }
  { MF_SA(sA[1][3], kf[4], 240); l[2] = fadd_s(l[2], sB[2][2][0]); PIN(l[2]); l[2] = fadd_s(l[2], sB[2][2][1]); PIN(l[2]); SBAR(); }
  { MF_SV(sA[2][3], kf[4], qf2[4]); pw[2][1][0] = cvtpk(sB[2][2][0], sB[2][2][1]); PIN(pw[2][1][0]); SBAR(); }
  { MF_SA(sA[0][3], kf[5], 212); vf[3] = VRD(1, 0, 3); SBAR(); }
  { MF_SA(sA[1][3], kf[5], 244); l[2] = fadd_s(l[2], sB[2][2][2]); PIN(l[2]); l[2] = fadd_s(l[2], sB[2][2][3]); PIN(l[2]); SBAR(); }
  { MF_SV(sA[2][3], kf[5], qf2[5]); pw[2][1][1] = cvtpk(sB[2][2][2], sB[2][2][3]); PIN(pw[2][1][1]); SBAR(); }
  { MF_SA(sA[0][3], kf[6], 216); vf[4] = VRD(1, 0, 4); SBAR(); }
  { MF_SA(sA[1][3], kf[6], 248); l[2] = fadd_s(l[2], sB[2][3][0]); PIN(l[2]); l[2] = fadd_s(l[2], sB[2][3][1]); PIN(l[2]); SBAR(); }
  { MF_SV(sA[2][3], kf[6], qf2[6]); pw[2][1][2] = cvtpk(sB[2][3][0], sB[2][3][1]); PIN(pw[2][1][2]); SBAR(); }
  { MF_SA(sA[0][3], kf[7], 220); vf[5] = VRD(1, 0, 5); SBAR(); }
  { MF_SA(sA[1][3], kf[7], 252); l[2] = fadd_s(l[2], sB[2][3][2]); PIN(l[2]); l[2] = fadd_s(l[2], sB[2][3][3]); PIN(l[2]); SBAR(); }
  { MF_SV(sA[2][3], kf[7], qf2[7]); pw[2][1][3] = cvtpk(sB[2][3][2], sB[2][3][3]); PIN(pw[2][1][3]); SBAR(); }
  _Pragma("unroll") for (int sb = 0; sb < 3; ++sb) { pf[sb][0] = __builtin_bit_cast(bf16x8, pw[sb][0]); pf[sb][1] = __builtin_bit_cast(bf16x8, pw[sb][1]); PIN(pf[sb][0]); PIN(pf[sb][1]); }
  asm volatile("s_nop 1" ::: "memory"); SBAR();
  asm volatile("s_waitcnt vmcnt(16)\n\ts_barrier" ::: "memory");
  { MF_O(0, vf[0], pf[0][0]); vf[6] = VRD(1, 0, 6); SBAR(); }
  { MF_O(64, vf[0], pf[1][0]); mx[0] = max2f(sA[0][0][0], sA[0][0][1]); PIN(mx[0]); SBAR(); }
  { MF_O(128, vf[0], pf[2][0]); mx[1] = max2f(sA[1][0][0], sA[1][0][1]); PIN(mx[1]); SBAR(); }
  { MF_O(4, vf[1], pf[0][0]); DMA_K1(t + 4, 0, 0); vf[7] = VRD(1, 0, 7); SBAR(); }
  { MF_O(68, vf[1], pf[1][0]); mx[2] = max2f(sA[2][0][0], sA[2][0][1]); PIN(mx[2]); SBAR(); }
  { MF_O(132, vf[1], pf[2][0]); mx[0] = max3f(mx[0], sA[0][0][2], sA[0][0][3]); PIN(mx[0]); SBAR(); }
  { MF_O(8, vf[2], pf[0][0]); vf[0] = VRD(1, 0, 8); SBAR(); }
  { MF_O(72, vf[2], pf[1][0]); mx[1] = max3f(mx[1], sA[1][0][2], sA[1][0][3]); PIN(mx[1]); SBAR(); }
  { MF_O(136, vf[2], pf[2][0]); mx[2] = max3f(mx[2], sA[2][0][2], sA[2][0][3]); PIN(mx[2]); SBAR(); }
  { MF_O(12, vf[3], pf[0][0]); DMA_K1(t + 4, 0, 1); vf[1] = VRD(1, 0, 9); SBAR(); }
  { MF_O(76, vf[3], pf[1][0]); mx[0] = max3f(mx[0], sA[0][1][0], sA[0][1][1]); PIN(mx[0]); SBAR(); }
  { MF_O(140, vf[3], pf[2][0]); mx[1] = max3f(mx[1], sA[1][1][0], sA[1][1][1]); PIN(mx[1]); SBAR(); }
  { MF_O(16, vf[4], pf[0][0]); vf[2] = VRD(1, 0, 10); SBAR(); }
  { MF_O(80, vf[4], pf[1][0]); mx[2] = max3f(mx[2], sA[2][1][0], sA[2][1][1]); PIN(mx[2]); SBAR(); }
  { MF_O(144, vf[4], pf[2][0]); mx[0] = max3f(mx[0], sA[0][1][2], sA[0][1][3]); PIN(mx[0]); SBAR(); }
  { MF_O(20, vf[5], pf[0][0]); DMA_K1(t + 4, 0, 2); vf[3] = VRD(1, 0, 11); SBAR(); }
  { MF_O(84, vf[5], pf[1][0]); mx[1] = max3f(mx[1], sA[1][1][2], sA[1][1][3]); PIN(mx[1]); SBAR(); }
  { MF_O(148, vf[5], pf[2][0]); mx[2] = max3f(mx[2], sA[2][1][2], sA[2][1][3]); PIN(mx[2]); SBAR(); }
  { MF_O(24, vf[6], pf[0][0]); vf[4] = VRD(1, 0, 12); SBAR(); }
  { MF_O(88, vf[6], pf[1][0]); mx[0] = max3f(mx[0], sA[0][2][0], sA[0][2][1]); PIN(mx[0]); SBAR(); }
  { MF_O(152, vf[6], pf[2][0]); mx[1] = max3f(mx[1], sA[1][2][0], sA[1][2][1]); PIN(mx[1]); SBAR(); }
  { MF_O(28, vf[7], pf[0][0]); DMA_K1(t + 4, 0, 3); vf[5] = VRD(1, 0, 13); SBAR(); }
  { MF_O(92, vf[7], pf[1][0]); mx[2] = max3f(mx[2], sA[2][2][0], sA[2][2][1]); PIN(mx[2]); SBAR(); }
  { MF_O(156, vf[7], pf[2][0]); mx[0] = max3f(mx[0], sA[0][2][2], sA[0][2][3]); PIN(mx[0]); SBAR(); }
  { MF_O(32, vf[0], pf[0][0]); vf[6] = VRD(1, 0, 14); SBAR(); }
  { MF_O(96, vf[0], pf[1][0]); mx[1] = max3f(mx[1], sA[1][2][2], sA[1][2][3]); PIN(mx[1]); SBAR(); }
  { MF_O(160, vf[0], pf[2][0]); mx[2] = max3f(mx[2], sA[2][2][2], sA[2][2][3]); PIN(mx[2]); SBAR(); }
  { MF_O(36, vf[1], pf[0][0]); DMA_K1(t + 4, 0, 4); vf[7] = VRD(1, 0, 15); SBAR(); }
  { MF_O(100, vf[1], pf[1][0]); mx[0] = max3f(mx[0], sA[0][3][0], sA[0][3][1]); PIN(mx[0]); SBAR(); }
  { MF_O(164, vf[1], pf[2][0]); mx[1] = max3f(mx[1], sA[1][3][0], sA[1][3][1]); PIN(mx[1]); SBAR(); }
  { MF_O(40, vf[2], pf[0][0]); vf[0] = VRD(1, 1, 0); SBAR(); }
  { MF_O(104, vf[2], pf[1][0]); mx[2] = max3f(mx[2], sA[2][3][0], sA[2][3][1]); PIN(mx[2]); SBAR(); }
  { MF_O(168, vf[2], pf[2][0]); mx[0] = max3f(mx[0], sA[0][3][2], sA[0][3][3]); PIN(mx[0]); SBAR(); }
  { MF_O(44, vf[3], pf[0][0]); DMA_K1(t + 4, 0, 5); vf[1] = VRD(1, 1, 1); SBAR(); }
  { MF_O(108, vf[3], pf[1][0]); mx[1] = max3f(mx[1], sA[1][3][2], sA[1][3][3]); PIN(mx[1]); SBAR(); }
  { MF_O(172, vf[3], pf[2][0]); mx[2] = max3f(mx[2], sA[2][3][2], sA[2][3][3]); PIN(mx[2]); SBAR(); }
  { MF_O(48, vf[4], pf[0][0]); vf[2] = VRD(1, 1, 2); SBAR(); }
  { MF_O(112, vf[4], pf[1][0]); mx[0] = xmax16(mx[0]); PIN(mx[0]); SBAR(); }
  { MF_O(176, vf[4], pf[2][0]); mx[1] = xmax16(mx[1]); PIN(mx[1]); SBAR(); }
  { MF_O(52, vf[5], pf[0][0]); DMA_K1(t + 4, 0, 6); vf[3] = VRD(1, 1, 3); SBAR(); }
  { MF_O(116, vf[5], pf[1][0]); mx[2] = xmax16(mx[2]); PIN(mx[2]); SBAR(); }
  { MF_O(180, vf[5], pf[2][0]); mx[0] = xmax32(mx[0]); PIN(mx[0]); SBAR(); }
  { MF_O(56, vf[6], pf[0][0]); vf[4] = VRD(1, 1, 4); SBAR(); }
  { MF_O(120, vf[6], pf[1][0]); mx[1] = xmax32(mx[1]); PIN(mx[1]); SBAR(); }
  { MF_O(184, vf[6], pf[2][0]); mx[2] = xmax32(mx[2]); PIN(mx[2]); SBAR(); }
  { MF_O(60, vf[7], pf[0][0]); DMA_K1(t + 4, 0, 7); vf[5] = VRD(1, 1, 5); SBAR(); }
  {
    const bool grow = (mx[0] - m[0] > THR) | (mx[1] - m[1] > THR) | (mx[2] - m[2] > THR);
    if (__builtin_expect(__any((int)grow), 0)) {
#pragma unroll
      for (int sb = 0; sb < 3; ++sb) { const float mn = fmaxf(m[sb], mx[sb]);
        const float al = __builtin_amdgcn_exp2f((m[sb] - mn) * CSC); m[sb] = mn; nmc[sb] = -mn * CSC; l[sb] *= al; alv[sb] = al; }
      pend = 1;
    }
    SBAR();
  }
  { MF_O(124, vf[7], pf[1][0]); sA[0][0][0] = exp_s(ffma_s(sA[0][0][0], csc, nmc[0])); PIN(sA[0][0][0]); SBAR(); }
  { MF_O(188, vf[7], pf[2][0]); sA[0][0][1] = exp_s(ffma_s(sA[0][0][1], csc, nmc[0])); PIN(sA[0][0][1]); SBAR(); }
  { MF_O(0, vf[0], pf[0][1]); vf[6] = VRD(1, 1, 6); sA[0][0][2] = exp_s(ffma_s(sA[0][0][2], csc, nmc[0])); PIN(sA[0][0][2]); SBAR(); }
  { MF_O(64, vf[0], pf[1][1]); sA[0][0][3] = exp_s(ffma_s(sA[0][0][3], csc, nmc[0])); PIN(sA[0][0][3]); SBAR(); }
  { MF_O(128, vf[0], pf[2][1]); sA[0][1][0] = exp_s(ffma_s(sA[0][1][0], csc, nmc[0])); PIN(sA[0][1][0]); SBAR(); }
  { MF_O(4, vf[1], pf[0][1]); vf[7] = VRD(1, 1, 7); sA[0][1][1] = exp_s(ffma_s(sA[0][1][1], csc, nmc[0])); PIN(sA[0][1][1]); SBAR(); }
  { MF_O(68, vf[1], pf[1][1]); sA[0][1][2] = exp_s(ffma_s(sA[0][1][2], csc, nmc[0])); PIN(sA[0][1][2]); SBAR(); }
  { MF_O(132, vf[1], pf[2][1]); sA[0][1][3] = exp_s(ffma_s(sA[0][1][3], csc, nmc[0])); PIN(sA[0][1][3]); SBAR(); }
  { MF_O(8, vf[2], pf[0][1]); vf[0] = VRD(1, 1, 8); sA[0][2][0] = exp_s(ffma_s(sA[0][2][0], csc, nmc[0])); PIN(sA[0][2][0]); SBAR(); }
  { MF_O(72, vf[2], pf[1][1]); sA[0][2][1] = exp_s(ffma_s(sA[0][2][1], csc, nmc[0])); PIN(sA[0][2][1]); SBAR(); }
  { MF_O(136, vf[2], pf[2][1]); sA[0][2][2] = exp_s(ffma_s(sA[0][2][2], csc, nmc[0])); PIN(sA[0][2][2]); SBAR(); }
  { MF_O(12, vf[3], pf[0][1]); vf[1] = VRD(1, 1, 9); sA[0][2][3] = exp_s(ffma_s(sA[0][2][3], csc, nmc[0])); PIN(sA[0][2][3]); SBAR(); }
  { MF_O(76, vf[3], pf[1][1]); sA[0][3][0] = exp_s(ffma_s(sA[0][3][0], csc, nmc[0])); PIN(sA[0][3][0]); SBAR(); }
  { MF_O(140, vf[3], pf[2][1]); sA[0][3][1] = exp_s(ffma_s(sA[0][3][1], csc, nmc[0])); PIN(sA[0][3][1]); SBAR(); }
  { MF_O(16, vf[4], pf[0][1]); vf[2] = VRD(1, 1, 10); sA[0][3][2] = exp_s(ffma_s(sA[0][3][2], csc, nmc[0])); PIN(sA[0][3][2]); SBAR(); }
  { MF_O(80, vf[4], pf[1][1]); sA[0][3][3] = exp_s(ffma_s(sA[0][3][3], csc, nmc[0])); PIN(sA[0][3][3]); SBAR(); }
  { MF_O(144, vf[4], pf[2][1]); sA[1][0][0] = exp_s(ffma_s(sA[1][0][0], csc, nmc[1])); PIN(sA[1][0][0]); SBAR(); }
  { MF_O(20, vf[5], pf[0][1]); vf[3] = VRD(1, 1, 11); sA[1][0][1] = exp_s(ffma_s(sA[1][0][1], csc, nmc[1])); PIN(sA[1][0][1]); SBAR(); }
  { MF_O(84, vf[5], pf[1][1]); sA[1][0][2] = exp_s(ffma_s(sA[1][0][2], csc, nmc[1])); PIN(sA[1][0][2]); SBAR(); }
  { MF_O(148, vf[5], pf[2][1]); sA[1][0][3] = exp_s(ffma_s(sA[1][0][3], csc, nmc[1])); PIN(sA[1][0][3]); SBAR(); }
  { MF_O(24, vf[6], pf[0][1]); vf[4] = VRD(1, 1, 12); SBAR(); }
  { MF_O(88, vf[6], pf[1][1]); sA[1][1][0] = exp_s(ffma_s(sA[1][1][0], csc, nmc[1])); PIN(sA[1][1][0]); SBAR(); }
  { MF_O(152, vf[6], pf[2][1]); sA[1][1][1] = exp_s(ffma_s(sA[1][1][1], csc, nmc[1])); PIN(sA[1][1][1]); SBAR(); }
  { MF_O(28, vf[7], pf[0][1]); vf[5] = VRD(1, 1, 13); SBAR(); }
  { MF_O(92, vf[7], pf[1][1]); sA[1][1][2] = exp_s(ffma_s(sA[1][1][2], csc, nmc[1])); PIN(sA[1][1][2]); SBAR(); }
  { MF_O(156, vf[7], pf[2][1]); sA[1][1][3] = exp_s(ffma_s(sA[1][1][3], csc, nmc[1])); PIN(sA[1][1][3]); SBAR(); }
  { MF_O(32, vf[0], pf[0][1]); vf[6] = VRD(1, 1, 14); SBAR(); }
  { MF_O(96, vf[0], pf[1][1]); sA[1][2][0] = exp_s(ffma_s(sA[1][2][0], csc, nmc[1])); PIN(sA[1][2][0]); SBAR(); }
  { MF_O(160, vf[0], pf[2][1]); sA[1][2][1] = exp_s(ffma_s(sA[1][2][1], csc, nmc[1])); PIN(sA[1][2][1]); SBAR(); }
  { MF_O(36, vf[1], pf[0][1]); vf[7] = VRD(1, 1, 15); SBAR(); }
  { MF_O(100, vf[1], pf[1][1]); sA[1][2][2] = exp_s(ffma_s(sA[1][2][2], csc, nmc[1])); PIN(sA[1][2][2]); SBAR(); }
  { MF_O(164, vf[1], pf[2][1]); sA[1][2][3] = exp_s(ffma_s(sA[1][2][3], csc, nmc[1])); PIN(sA[1][2][3]); SBAR(); }
  { MF_O(40, vf[2], pf[0][1]);  SBAR(); }
  { MF_O(104, vf[2], pf[1][1]); sA[1][3][0] = exp_s(ffma_s(sA[1][3][0], csc, nmc[1])); PIN(sA[1][3][0]); SBAR(); }
  { MF_O(168, vf[2], pf[2][1]); sA[1][3][1] = exp_s(ffma_s(sA[1][3][1], csc, nmc[1])); PIN(sA[1][3][1]); SBAR(); }
  { MF_O(44, vf[3], pf[0][1]);  SBAR(); }
  { MF_O(108, vf[3], pf[1][1]); sA[1][3][2] = exp_s(ffma_s(sA[1][3][2], csc, nmc[1])); PIN(sA[1][3][2]); SBAR(); }
  { MF_O(172, vf[3], pf[2][1]); sA[1][3][3] = exp_s(ffma_s(sA[1][3][3], csc, nmc[1])); PIN(sA[1][3][3]); SBAR(); }
  { MF_O(48, vf[4], pf[0][1]);  SBAR(); }
  { MF_O(112, vf[4], pf[1][1]); sA[2][0][0] = exp_s(ffma_s(sA[2][0][0], csc, nmc[2])); PIN(sA[2][0][0]); SBAR(); }
  { MF_O(176, vf[4], pf[2][1]); sA[2][0][1] = exp_s(ffma_s(sA[2][0][1], csc, nmc[2])); PIN(sA[2][0][1]); SBAR(); }
  { MF_O(52, vf[5], pf[0][1]);  SBAR(); }
  { MF_O(116, vf[5], pf[1][1]); sA[2][0][2] = exp_s(ffma_s(sA[2][0][2], csc, nmc[2])); PIN(sA[2][0][2]); SBAR(); }
  { MF_O(180, vf[5], pf[2][1]); sA[2][0][3] = exp_s(ffma_s(sA[2][0][3], csc, nmc[2])); PIN(sA[2][0][3]); SBAR(); }
  { MF_O(56, vf[6], pf[0][1]);  SBAR(); }
  { MF_O(120, vf[6], pf[1][1]); sA[2][1][0] = exp_s(ffma_s(sA[2][1][0], csc, nmc[2])); PIN(sA[2][1][0]); SBAR(); }
  { MF_O(184, vf[6], pf[2][1]); sA[2][1][1] = exp_s(ffma_s(sA[2][1][1], csc, nmc[2])); PIN(sA[2][1][1]); SBAR(); }
  { MF_O(60, vf[7], pf[0][1]);  SBAR(); }
  { MF_O(124, vf[7], pf[1][1]); sA[2][1][2] = exp_s(ffma_s(sA[2][1][2], csc, nmc[2])); PIN(sA[2][1][2]); SBAR(); }
  { MF_O(188, vf[7], pf[2][1]); sA[2][1][3] = exp_s(ffma_s(sA[2][1][3], csc, nmc[2])); PIN(sA[2][1][3]); SBAR(); }
  if (__builtin_expect(pend, 0)) {
    asm volatile("s_nop 7\n\ts_nop 7" ::: "memory");
      { float t0, t1; asm volatile("v_accvgpr_read_b32 %0, a0\n\tv_mul_f32 %0, %2, %0\n\tv_accvgpr_write_b32 a0, %0\n\t" "v_accvgpr_read_b32 %1, a1\n\tv_mul_f32 %1, %2, %1\n\tv_accvgpr_write_b32 a1, %1\n\t" "v_accvgpr_read_b32 %0, a2\n\tv_mul_f32 %0, %2, %0\n\tv_accvgpr_write_b32 a2, %0\n\t" "v_accvgpr_read_b32 %1, a3\n\tv_mul_f32 %1, %2, %1\n\tv_accvgpr_write_b32 a3, %1\n\t" "v_accvgpr_read_b32 %0, a4\n\tv_mul_f32 %0, %2, %0\n\tv_accvgpr_write_b32 a4, %0\n\t" "v_accvgpr_read_b32 %1, a5\n\tv_mul_f32 %1, %2, %1\n\tv_accvgpr_write_b32 a5, %1\n\t" "v_accvgpr_read_b32 %0, a6\n\tv_mul_f32 %0, %2, %0\n\tv_accvgpr_write_b32 a6, %0\n\t" "v_accvgpr_read_b32 %1, a7\n\tv_mul_f32 %1, %2, %1\n\tv_accvgpr_write_b32 a7, %1\n\t" "v_accvgpr_read_b32 %0, a8\n\tv_mul_f32 %0, %2, %0\n\tv_accvgpr_write_b32 a8, %0\n\t" "v_accvgpr_read_b32 %1, a9\n\tv_mul_f32 %1, %2, %1\n\tv_accvgpr_write_b32 a9, %1\n\t" "v_accvgpr_read_b32 %0, a10\n\tv_mul_f32 %0, %2, %0\n\tv_accvgpr_write_b32 a10, %0\n\t" "v_accvgpr_read_b32 %1, a11\n\tv_mul_f32 %1, %2, %1\n\tv_accvgpr_write_b32 a11, %1\n\t" "v_accvgpr_read_b32 %0, a12\n\tv_mul_f32 %0, %2, %0\n\tv_accvgpr_write_b32 a12, %0\n\t" "v_accvgpr_read_b32 %1, a13\n\tv_mul_f32 %1, %2, %1\n\tv_accvgpr_write_b32 a13, %1\n\t" "v_accvgpr_read_b32 %0, a14\n\tv_mul_f32 %0, %2, %0\n\tv_accvgpr_write_b32 a14, %0\n\t" "v_accvgpr_read_b32 %1, a15\n\tv_mul_f32 %1, %2, %1\n\tv_accvgpr_write_b32 a15, %1\n\t" "v_accvgpr_read_b32 %0, a16\n\tv_mul_f32 %0, %2, %0\n\tv_accvgpr_write_b32 a16, %0\n\t" "v_accvgpr_read_b32 %1, a17\n\tv_mul_f32 %1, %2, %1\n\tv_accvgpr_write_b32 a17, %1\n\t" "v_accvgpr_read_b32 %0, a18\n\tv_mul_f32 %0, %2, %0\n\tv_accvgpr_write_b32 a18, %0\n\t" "v_accvgpr_read_b32 %1, a19\n\tv_mul_f32 %1, %2, %1\n\tv_accvgpr_write_b32 a19, %1\n\t" "v_accvgpr_read_b32 %0, a20\n\tv_mul_f32 %0, %2, %0\n\tv_accvgpr_write_b32 a20, %0\n\t" "v_accvgpr_read_b32 %1, a21\n\tv_mul_f32 %1, %2, %1\n\tv_accvgpr_write_b32 a21, %1\n\t" "v_accvgpr_read_b32 %0, a22\n\tv_mul_f32 %0, %2, %0\n\tv_accvgpr_write_b32 a22, %0\n\t" "v_accvgpr_read_b32 %1, a23\n\tv_mul_f32 %1, %2, %1\n\tv_accvgpr_write_b32 a23, %1\n\t" "v_accvgpr_read_b32 %0, a24\n\tv_mul_f32 %0, %2, %0\n\tv_accvgpr_write_b32 a24, %0\n\t" "v_accvgpr_read_b32 %1, a25\n\tv_mul_f32 %1, %2, %1\n\tv_accvgpr_write_b32 a25, %1\n\t" "v_accvgpr_read_b32 %0, a26\n\tv_mul_f32 %0, %2, %0\n\tv_accvgpr_write_b32 a26, %0\n\t" "v_accvgpr_read_b32 %1, a27\n\tv_mul_f32 %1, %2, %1\n\tv_accvgpr_write_b32 a27, %1\n\t" "v_accvgpr_read_b32 %0, a28\n\tv_mul_f32 %0, %2, %0\n\tv_accvgpr_write_b32 a28, %0\n\t" "v_accvgpr_read_b32 %1, a29\n\tv_mul_f32 %1, %2, %1\n\tv_accvgpr_write_b32 a29, %1\n\t" "v_accvgpr_read_b32 %0, a30\n\tv_mul_f32 %0, %2, %0\n\tv_accvgpr_write_b32 a30, %0\n\t" "v_accvgpr_read_b32 %1, a31\n\tv_mul_f32 %1, %2, %1\n\tv_accvgpr_write_b32 a31, %1\n\t" "v_accvgpr_read_b32 %0, a32\n\tv_mul_f32 %0, %2, %0\n\tv_accvgpr_write_b32 a32, %0\n\t" "v_accvgpr_read_b32 %1, a33\n\tv_mul_f32 %1, %2, %1\n\tv_accvgpr_write_b32 a33, %1\n\t" "v_accvgpr_read_b32 %0, a34\n\tv_mul_f32 %0, %2, %0\n\tv_accvgpr_write_b32 a34, %0\n\t" "v_accvgpr_read_b32 %1, a35\n\tv_mul_f32 %1, %2, %1\n\tv_accvgpr_write_b32 a35, %1\n\t" "v_accvgpr_read_b32 %0, a36\n\tv_mul_f32 %0, %2, %0\n\tv_accvgpr_write_b32 a36, %0\n\t" "v_accvgpr_read_b32 %1, a37\n\tv_mul_f32 %1, %2, %1\n\tv_accvgpr_write_b32 a37, %1\n\t" "v_accvgpr_read_b32 %0, a38\n\tv_mul_f32 %0, %2, %0\n\tv_accvgpr_write_b32 a38, %0\n\t" "v_accvgpr_read_b32 %1, a39\n\tv_mul_f32 %1, %2, %1\n\tv_accvgpr_write_b32 a39, %1\n\t" "v_accvgpr_read_b32 %0, a40\n\tv_mul_f32 %0, %2, %0\n\tv_accvgpr_write_b32 a40, %0\n\t" "v_accvgpr_read_b32 %1, a41\n\tv_mul_f32 %1, %2, %1\n\tv_accvgpr_write_b32 a41, %1\n\t" "v_accvgpr_read_b32 %0, a42\n\tv_mul_f32 %0, %2, %0\n\tv_accvgpr_write_b32 a42, %0\n\t" "v_accvgpr_read_b32 %1, a43\n\tv_mul_f32 %1, %2, %1\n\tv_accvgpr_write_b32 a43, %1\n\t" "v_accvgpr_read_b32 %0, a44\n\tv_mul_f32 %0, %2, %0\n\tv_accvgpr_write_b32 a44, %0\n\t" "v_accvgpr_read_b32 %1, a45\n\tv_mul_f32 %1, %2, %1\n\tv_accvgpr_write_b32 a45, %1\n\t" "v_accvgpr_read_b32 %0, a46\n\tv_mul_f32 %0, %2, %0\n\tv_accvgpr_write_b32 a46, %0\n\t" "v_accvgpr_read_b32 %1, a47\n\tv_mul_f32 %1, %2, %1\n\tv_accvgpr_write_b32 a47, %1\n\t" "v_accvgpr_read_b32 %0, a48\n\tv_mul_f32 %0, %2, %0\n\tv_accvgpr_write_b32 a48, %0\n\t" "v_accvgpr_read_b32 %1, a49\n\tv_mul_f32 %1, %2, %1\n\tv_accvgpr_write_b32 a49, %1\n\t" "v_accvgpr_read_b32 %0, a50\n\tv_mul_f32 %0, %2, %0\n\tv_accvgpr_write_b32 a50, %0\n\t" "v_accvgpr_read_b32 %1, a51\n\tv_mul_f32 %1, %2, %1\n\tv_accvgpr_write_b32 a51, %1\n\t" "v_accvgpr_read_b32 %0, a52\n\tv_mul_f32 %0, %2, %0\n\tv_accvgpr_write_b32 a52, %0\n\t" "v_accvgpr_read_b32 %1, a53\n\tv_mul_f32 %1, %2, %1\n\tv_accvgpr_write_b32 a53, %1\n\t" "v_accvgpr_read_b32 %0, a54\n\tv_mul_f32 %0, %2, %0\n\tv_accvgpr_write_b32 a54, %0\n\t" "v_accvgpr_read_b32 %1, a55\n\tv_mul_f32 %1, %2, %1\n\tv_accvgpr_write_b32 a55, %1\n\t" "v_accvgpr_read_b32 %0, a56\n\tv_mul_f32 %0, %2, %0\n\tv_accvgpr_write_b32 a56, %0\n\t" "v_accvgpr_read_b32 %1, a57\n\tv_mul_f32 %1, %2, %1\n\tv_accvgpr_write_b32 a57, %1\n\t" "v_accvgpr_read_b32 %0, a58\n\tv_mul_f32 %0, %2, %0\n\tv_accvgpr_write_b32 a58, %0\n\t" "v_accvgpr_read_b32 %1, a59\n\tv_mul_f32 %1, %2, %1\n\tv_accvgpr_write_b32 a59, %1\n\t" "v_accvgpr_read_b32 %0, a60\n\tv_mul_f32 %0, %2, %0\n\tv_accvgpr_write_b32 a60, %0\n\t" "v_accvgpr_read_b32 %1, a61\n\tv_mul_f32 %1, %2, %1\n\tv_accvgpr_write_b32 a61, %1\n\t" "v_accvgpr_read_b32 %0, a62\n\tv_mul_f32 %0, %2, %0\n\tv_accvgpr_write_b32 a62, %0\n\t" "v_accvgpr_read_b32 %1, a63\n\tv_mul_f32 %1, %2, %1\n\tv_accvgpr_write_b32 a63, %1\n\t" : "=&v"(t0), "=&v"(t1) : "v"(alv[0]) : ACLOB); }
      { float t0, t1; asm volatile("v_accvgpr_read_b32 %0, a64\n\tv_mul_f32 %0, %2, %0\n\tv_accvgpr_write_b32 a64, %0\n\t" "v_accvgpr_read_b32 %1, a65\n\tv_mul_f32 %1, %2, %1\n\tv_accvgpr_write_b32 a65, %1\n\t" "v_accvgpr_read_b32 %0, a66\n\tv_mul_f32 %0, %2, %0\n\tv_accvgpr_write_b32 a66, %0\n\t" "v_accvgpr_read_b32 %1, a67\n\tv_mul_f32 %1, %2, %1\n\tv_accvgpr_write_b32 a67, %1\n\t" "v_accvgpr_read_b32 %0, a68\n\tv_mul_f32 %0, %2, %0\n\tv_accvgpr_write_b32 a68, %0\n\t" "v_accvgpr_read_b32 %1, a69\n\tv_mul_f32 %1, %2, %1\n\tv_accvgpr_write_b32 a69, %1\n\t" "v_accvgpr_read_b32 %0, a70\n\tv_mul_f32 %0, %2, %0\n\tv_accvgpr_write_b32 a70, %0\n\t" "v_accvgpr_read_b32 %1, a71\n\tv_mul_f32 %1, %2, %1\n\tv_accvgpr_write_b32 a71, %1\n\t" "v_accvgpr_read_b32 %0, a72\n\tv_mul_f32 %0, %2, %0\n\tv_accvgpr_write_b32 a72, %0\n\t" "v_accvgpr_read_b32 %1, a73\n\tv_mul_f32 %1, %2, %1\n\tv_accvgpr_write_b32 a73, %1\n\t" "v_accvgpr_read_b32 %0, a74\n\tv_mul_f32 %0, %2, %0\n\tv_accvgpr_write_b32 a74, %0\n\t" "v_accvgpr_read_b32 %1, a75\n\tv_mul_f32 %1, %2, %1\n\tv_accvgpr_write_b32 a75, %1\n\t" "v_accvgpr_read_b32 %0, a76\n\tv_mul_f32 %0, %2, %0\n\tv_accvgpr_write_b32 a76, %0\n\t" "v_accvgpr_read_b32 %1, a77\n\tv_mul_f32 %1, %2, %1\n\tv_accvgpr_write_b32 a77, %1\n\t" "v_accvgpr_read_b32 %0, a78\n\tv_mul_f32 %0, %2, %0\n\tv_accvgpr_write_b32 a78, %0\n\t" "v_accvgpr_read_b32 %1, a79\n\tv_mul_f32 %1, %2, %1\n\tv_accvgpr_write_b32 a79, %1\n\t" "v_accvgpr_read_b32 %0, a80\n\tv_mul_f32 %0, %2, %0\n\tv_accvgpr_write_b32 a80, %0\n\t" "v_accvgpr_read_b32 %1, a81\n\tv_mul_f32 %1, %2, %1\n\tv_accvgpr_write_b32 a81, %1\n\t" "v_accvgpr_read_b32 %0, a82\n\tv_mul_f32 %0, %2, %0\n\tv_accvgpr_write_b32 a82, %0\n\t" "v_accvgpr_read_b32 %1, a83\n\tv_mul_f32 %1, %2, %1\n\tv_accvgpr_write_b32 a83, %1\n\t" "v_accvgpr_read_b32 %0, a84\n\tv_mul_f32 %0, %2, %0\n\tv_accvgpr_write_b32 a84, %0\n\t" "v_accvgpr_read_b32 %1, a85\n\tv_mul_f32 %1, %2, %1\n\tv_accvgpr_write_b32 a85, %1\n\t" "v_accvgpr_read_b32 %0, a86\n\tv_mul_f32 %0, %2, %0\n\tv_accvgpr_write_b32 a86, %0\n\t" "v_accvgpr_read_b32 %1, a87\n\tv_mul_f32 %1, %2, %1\n\tv_accvgpr_write_b32 a87, %1\n\t" "v_accvgpr_read_b32 %0, a88\n\tv_mul_f32 %0, %2, %0\n\tv_accvgpr_write_b32 a88, %0\n\t" "v_accvgpr_read_b32 %1, a89\n\tv_mul_f32 %1, %2, %1\n\tv_accvgpr_write_b32 a89, %1\n\t" "v_accvgpr_read_b32 %0, a90\n\tv_mul_f32 %0, %2, %0\n\tv_accvgpr_write_b32 a90, %0\n\t" "v_accvgpr_read_b32 %1, a91\n\tv_mul_f32 %1, %2, %1\n\tv_accvgpr_write_b32 a91, %1\n\t" "v_accvgpr_read_b32 %0, a92\n\tv_mul_f32 %0, %2, %0\n\tv_accvgpr_write_b32 a92, %0\n\t" "v_accvgpr_read_b32 %1, a93\n\tv_mul_f32 %1, %2, %1\n\tv_accvgpr_write_b32 a93, %1\n\t" "v_accvgpr_read_b32 %0, a94\n\tv_mul_f32 %0, %2, %0\n\tv_accvgpr_write_b32 a94, %0\n\t" "v_accvgpr_read_b32 %1, a95\n\tv_mul_f32 %1, %2, %1\n\tv_accvgpr_write_b32 a95, %1\n\t" "v_accvgpr_read_b32 %0, a96\n\tv_mul_f32 %0, %2, %0\n\tv_accvgpr_write_b32 a96, %0\n\t" "v_accvgpr_read_b32 %1, a97\n\tv_mul_f32 %1, %2, %1\n\tv_accvgpr_write_b32 a97, %1\n\t" "v_accvgpr_read_b32 %0, a98\n\tv_mul_f32 %0, %2, %0\n\tv_accvgpr_write_b32 a98, %0\n\t" "v_accvgpr_read_b32 %1, a99\n\tv_mul_f32 %1, %2, %1\n\tv_accvgpr_write_b32 a99, %1\n\t" "v_accvgpr_read_b32 %0, a100\n\tv_mul_f32 %0, %2, %0\n\tv_accvgpr_write_b32 a100, %0\n\t" "v_accvgpr_read_b32 %1, a101\n\tv_mul_f32 %1, %2, %1\n\tv_accvgpr_write_b32 a101, %1\n\t" "v_accvgpr_read_b32 %0, a102\n\tv_mul_f32 %0, %2, %0\n\tv_accvgpr_write_b32 a102, %0\n\t" "v_accvgpr_read_b32 %1, a103\n\tv_mul_f32 %1, %2, %1\n\tv_accvgpr_write_b32 a103, %1\n\t" "v_accvgpr_read_b32 %0, a104\n\tv_mul_f32 %0, %2, %0\n\tv_accvgpr_write_b32 a104, %0\n\t" "v_accvgpr_read_b32 %1, a105\n\tv_mul_f32 %1, %2, %1\n\tv_accvgpr_write_b32 a105, %1\n\t" "v_accvgpr_read_b32 %0, a106\n\tv_mul_f32 %0, %2, %0\n\tv_accvgpr_write_b32 a106, %0\n\t" "v_accvgpr_read_b32 %1, a107\n\tv_mul_f32 %1, %2, %1\n\tv_accvgpr_write_b32 a107, %1\n\t" "v_accvgpr_read_b32 %0, a108\n\tv_mul_f32 %0, %2, %0\n\tv_accvgpr_write_b32 a108, %0\n\t" "v_accvgpr_read_b32 %1, a109\n\tv_mul_f32 %1, %2, %1\n\tv_accvgpr_write_b32 a109, %1\n\t" "v_accvgpr_read_b32 %0, a110\n\tv_mul_f32 %0, %2, %0\n\tv_accvgpr_write_b32 a110, %0\n\t" "v_accvgpr_read_b32 %1, a111\n\tv_mul_f32 %1, %2, %1\n\tv_accvgpr_write_b32 a111, %1\n\t" "v_accvgpr_read_b32 %0, a112\n\tv_mul_f32 %0, %2, %0\n\tv_accvgpr_write_b32 a112, %0\n\t" "v_accvgpr_read_b32 %1, a113\n\tv_mul_f32 %1, %2, %1\n\tv_accvgpr_write_b32 a113, %1\n\t" "v_accvgpr_read_b32 %0, a114\n\tv_mul_f32 %0, %2, %0\n\tv_accvgpr_write_b32 a114, %0\n\t" "v_accvgpr_read_b32 %1, a115\n\tv_mul_f32 %1, %2, %1\n\tv_accvgpr_write_b32 a115, %1\n\t" "v_accvgpr_read_b32 %0, a116\n\tv_mul_f32 %0, %2, %0\n\tv_accvgpr_write_b32 a116, %0\n\t" "v_accvgpr_read_b32 %1, a117\n\tv_mul_f32 %1, %2, %1\n\tv_accvgpr_write_b32 a117, %1\n\t" "v_accvgpr_read_b32 %0, a118\n\tv_mul_f32 %0, %2, %0\n\tv_accvgpr_write_b32 a118, %0\n\t" "v_accvgpr_read_b32 %1, a119\n\tv_mul_f32 %1, %2, %1\n\tv_accvgpr_write_b32 a119, %1\n\t" "v_accvgpr_read_b32 %0, a120\n\tv_mul_f32 %0, %2, %0\n\tv_accvgpr_write_b32 a120, %0\n\t" "v_accvgpr_read_b32 %1, a121\n\tv_mul_f32 %1, %2, %1\n\tv_accvgpr_write_b32 a121, %1\n\t" "v_accvgpr_read_b32 %0, a122\n\tv_mul_f32 %0, %2, %0\n\tv_accvgpr_write_b32 a122, %0\n\t" "v_accvgpr_read_b32 %1, a123\n\tv_mul_f32 %1, %2, %1\n\tv_accvgpr_write_b32 a123, %1\n\t" "v_accvgpr_read_b32 %0, a124\n\tv_mul_f32 %0, %2, %0\n\tv_accvgpr_write_b32 a124, %0\n\t" "v_accvgpr_read_b32 %1, a125\n\tv_mul_f32 %1, %2, %1\n\tv_accvgpr_write_b32 a125, %1\n\t" "v_accvgpr_read_b32 %0, a126\n\tv_mul_f32 %0, %2, %0\n\tv_accvgpr_write_b32 a126, %0\n\t" "v_accvgpr_read_b32 %1, a127\n\tv_mul_f32 %1, %2, %1\n\tv_accvgpr_write_b32 a127, %1\n\t" : "=&v"(t0), "=&v"(t1) : "v"(alv[1]) : ACLOB); }
      { float t0, t1; asm volatile("v_accvgpr_read_b32 %0, a128\n\tv_mul_f32 %0, %2, %0\n\tv_accvgpr_write_b32 a128, %0\n\t" "v_accvgpr_read_b32 %1, a129\n\tv_mul_f32 %1, %2, %1\n\tv_accvgpr_write_b32 a129, %1\n\t" "v_accvgpr_read_b32 %0, a130\n\tv_mul_f32 %0, %2, %0\n\tv_accvgpr_write_b32 a130, %0\n\t" "v_accvgpr_read_b32 %1, a131\n\tv_mul_f32 %1, %2, %1\n\tv_accvgpr_write_b32 a131, %1\n\t" "v_accvgpr_read_b32 %0, a132\n\tv_mul_f32 %0, %2, %0\n\tv_accvgpr_write_b32 a132, %0\n\t" "v_accvgpr_read_b32 %1, a133\n\tv_mul_f32 %1, %2, %1\n\tv_accvgpr_write_b32 a133, %1\n\t" "v_accvgpr_read_b32 %0, a134\n\tv_mul_f32 %0, %2, %0\n\tv_accvgpr_write_b32 a134, %0\n\t" "v_accvgpr_read_b32 %1, a135\n\tv_mul_f32 %1, %2, %1\n\tv_accvgpr_write_b32 a135, %1\n\t" "v_accvgpr_read_b32 %0, a136\n\tv_mul_f32 %0, %2, %0\n\tv_accvgpr_write_b32 a136, %0\n\t" "v_accvgpr_read_b32 %1, a137\n\tv_mul_f32 %1, %2, %1\n\tv_accvgpr_write_b32 a137, %1\n\t" "v_accvgpr_read_b32 %0, a138\n\tv_mul_f32 %0, %2, %0\n\tv_accvgpr_write_b32 a138, %0\n\t" "v_accvgpr_read_b32 %1, a139\n\tv_mul_f32 %1, %2, %1\n\tv_accvgpr_write_b32 a139, %1\n\t" "v_accvgpr_read_b32 %0, a140\n\tv_mul_f32 %0, %2, %0\n\tv_accvgpr_write_b32 a140, %0\n\t" "v_accvgpr_read_b32 %1, a141\n\tv_mul_f32 %1, %2, %1\n\tv_accvgpr_write_b32 a141, %1\n\t" "v_accvgpr_read_b32 %0, a142\n\tv_mul_f32 %0, %2, %0\n\tv_accvgpr_write_b32 a142, %0\n\t" "v_accvgpr_read_b32 %1, a143\n\tv_mul_f32 %1, %2, %1\n\tv_accvgpr_write_b32 a143, %1\n\t" "v_accvgpr_read_b32 %0, a144\n\tv_mul_f32 %0, %2, %0\n\tv_accvgpr_write_b32 a144, %0\n\t" "v_accvgpr_read_b32 %1, a145\n\tv_mul_f32 %1, %2, %1\n\tv_accvgpr_write_b32 a145, %1\n\t" "v_accvgpr_read_b32 %0, a146\n\tv_mul_f32 %0, %2, %0\n\tv_accvgpr_write_b32 a146, %0\n\t" "v_accvgpr_read_b32 %1, a147\n\tv_mul_f32 %1, %2, %1\n\tv_accvgpr_write_b32 a147, %1\n\t" "v_accvgpr_read_b32 %0, a148\n\tv_mul_f32 %0, %2, %0\n\tv_accvgpr_write_b32 a148, %0\n\t" "v_accvgpr_read_b32 %1, a149\n\tv_mul_f32 %1, %2, %1\n\tv_accvgpr_write_b32 a149, %1\n\t" "v_accvgpr_read_b32 %0, a150\n\tv_mul_f32 %0, %2, %0\n\tv_accvgpr_write_b32 a150, %0\n\t" "v_accvgpr_read_b32 %1, a151\n\tv_mul_f32 %1, %2, %1\n\tv_accvgpr_write_b32 a151, %1\n\t" "v_accvgpr_read_b32 %0, a152\n\tv_mul_f32 %0, %2, %0\n\tv_accvgpr_write_b32 a152, %0\n\t" "v_accvgpr_read_b32 %1, a153\n\tv_mul_f32 %1, %2, %1\n\tv_accvgpr_write_b32 a153, %1\n\t" "v_accvgpr_read_b32 %0, a154\n\tv_mul_f32 %0, %2, %0\n\tv_accvgpr_write_b32 a154, %0\n\t" "v_accvgpr_read_b32 %1, a155\n\tv_mul_f32 %1, %2, %1\n\tv_accvgpr_write_b32 a155, %1\n\t" "v_accvgpr_read_b32 %0, a156\n\tv_mul_f32 %0, %2, %0\n\tv_accvgpr_write_b32 a156, %0\n\t" "v_accvgpr_read_b32 %1, a157\n\tv_mul_f32 %1, %2, %1\n\tv_accvgpr_write_b32 a157, %1\n\t" "v_accvgpr_read_b32 %0, a158\n\tv_mul_f32 %0, %2, %0\n\tv_accvgpr_write_b32 a158, %0\n\t" "v_accvgpr_read_b32 %1, a159\n\tv_mul_f32 %1, %2, %1\n\tv_accvgpr_write_b32 a159, %1\n\t" "v_accvgpr_read_b32 %0, a160\n\tv_mul_f32 %0, %2, %0\n\tv_accvgpr_write_b32 a160, %0\n\t" "v_accvgpr_read_b32 %1, a161\n\tv_mul_f32 %1, %2, %1\n\tv_accvgpr_write_b32 a161, %1\n\t" "v_accvgpr_read_b32 %0, a162\n\tv_mul_f32 %0, %2, %0\n\tv_accvgpr_write_b32 a162, %0\n\t" "v_accvgpr_read_b32 %1, a163\n\tv_mul_f32 %1, %2, %1\n\tv_accvgpr_write_b32 a163, %1\n\t" "v_accvgpr_read_b32 %0, a164\n\tv_mul_f32 %0, %2, %0\n\tv_accvgpr_write_b32 a164, %0\n\t" "v_accvgpr_read_b32 %1, a165\n\tv_mul_f32 %1, %2, %1\n\tv_accvgpr_write_b32 a165, %1\n\t" "v_accvgpr_read_b32 %0, a166\n\tv_mul_f32 %0, %2, %0\n\tv_accvgpr_write_b32 a166, %0\n\t" "v_accvgpr_read_b32 %1, a167\n\tv_mul_f32 %1, %2, %1\n\tv_accvgpr_write_b32 a167, %1\n\t" "v_accvgpr_read_b32 %0, a168\n\tv_mul_f32 %0, %2, %0\n\tv_accvgpr_write_b32 a168, %0\n\t" "v_accvgpr_read_b32 %1, a169\n\tv_mul_f32 %1, %2, %1\n\tv_accvgpr_write_b32 a169, %1\n\t" "v_accvgpr_read_b32 %0, a170\n\tv_mul_f32 %0, %2, %0\n\tv_accvgpr_write_b32 a170, %0\n\t" "v_accvgpr_read_b32 %1, a171\n\tv_mul_f32 %1, %2, %1\n\tv_accvgpr_write_b32 a171, %1\n\t" "v_accvgpr_read_b32 %0, a172\n\tv_mul_f32 %0, %2, %0\n\tv_accvgpr_write_b32 a172, %0\n\t" "v_accvgpr_read_b32 %1, a173\n\tv_mul_f32 %1, %2, %1\n\tv_accvgpr_write_b32 a173, %1\n\t" "v_accvgpr_read_b32 %0, a174\n\tv_mul_f32 %0, %2, %0\n\tv_accvgpr_write_b32 a174, %0\n\t" "v_accvgpr_read_b32 %1, a175\n\tv_mul_f32 %1, %2, %1\n\tv_accvgpr_write_b32 a175, %1\n\t" "v_accvgpr_read_b32 %0, a176\n\tv_mul_f32 %0, %2, %0\n\tv_accvgpr_write_b32 a176, %0\n\t" "v_accvgpr_read_b32 %1, a177\n\tv_mul_f32 %1, %2, %1\n\tv_accvgpr_write_b32 a177, %1\n\t" "v_accvgpr_read_b32 %0, a178\n\tv_mul_f32 %0, %2, %0\n\tv_accvgpr_write_b32 a178, %0\n\t" "v_accvgpr_read_b32 %1, a179\n\tv_mul_f32 %1, %2, %1\n\tv_accvgpr_write_b32 a179, %1\n\t" "v_accvgpr_read_b32 %0, a180\n\tv_mul_f32 %0, %2, %0\n\tv_accvgpr_write_b32 a180, %0\n\t" "v_accvgpr_read_b32 %1, a181\n\tv_mul_f32 %1, %2, %1\n\tv_accvgpr_write_b32 a181, %1\n\t" "v_accvgpr_read_b32 %0, a182\n\tv_mul_f32 %0, %2, %0\n\tv_accvgpr_write_b32 a182, %0\n\t" "v_accvgpr_read_b32 %1, a183\n\tv_mul_f32 %1, %2, %1\n\tv_accvgpr_write_b32 a183, %1\n\t" "v_accvgpr_read_b32 %0, a184\n\tv_mul_f32 %0, %2, %0\n\tv_accvgpr_write_b32 a184, %0\n\t" "v_accvgpr_read_b32 %1, a185\n\tv_mul_f32 %1, %2, %1\n\tv_accvgpr_write_b32 a185, %1\n\t" "v_accvgpr_read_b32 %0, a186\n\tv_mul_f32 %0, %2, %0\n\tv_accvgpr_write_b32 a186, %0\n\t" "v_accvgpr_read_b32 %1, a187\n\tv_mul_f32 %1, %2, %1\n\tv_accvgpr_write_b32 a187, %1\n\t" "v_accvgpr_read_b32 %0, a188\n\tv_mul_f32 %0, %2, %0\n\tv_accvgpr_write_b32 a188, %0\n\t" "v_accvgpr_read_b32 %1, a189\n\tv_mul_f32 %1, %2, %1\n\tv_accvgpr_write_b32 a189, %1\n\t" "v_accvgpr_read_b32 %0, a190\n\tv_mul_f32 %0, %2, %0\n\tv_accvgpr_write_b32 a190, %0\n\t" "v_accvgpr_read_b32 %1, a191\n\tv_mul_f32 %1, %2, %1\n\tv_accvgpr_write_b32 a191, %1\n\t" : "=&v"(t0), "=&v"(t1) : "v"(alv[2]) : ACLOB); }
    asm volatile("s_nop 1" ::: "memory"); pend = 0;
  }
  SBAR();
  }
  asm volatile("s_waitcnt vmcnt(16)\n\ts_barrier" ::: "memory");
  kf[0] = KRD(1, 0, 0); kf[1] = KRD(1, 0, 1); kf[2] = KRD(1, 0, 2); kf[3] = KRD(1, 0, 3); kf[4] = KRD(1, 0, 4); kf[5] = KRD(1, 0, 5);
  { MF_S0A(sB[0][0], kf[0], 192); kf[6] = KRD(1, 0, 6); SBAR(); }
  { MF_S0A(sB[1][0], kf[0], 224); DMA_V1(NT - 3, 1, 0); SBAR(); }
  { MF_S0V(sB[2][0], kf[0], qf2[0]); sA[2][2][0] = exp_s(ffma_s(sA[2][2][0], csc, nmc[2])); PIN(sA[2][2][0]); SBAR(); }
  { MF_SA(sB[0][0], kf[1], 196); kf[7] = KRD(1, 0, 7); SBAR(); }
  { MF_SA(sB[1][0], kf[1], 228); DMA_V1(NT - 3, 1, 1); SBAR(); }
  { MF_SV(sB[2][0], kf[1], qf2[1]); sA[2][2][1] = exp_s(ffma_s(sA[2][2][1], csc, nmc[2])); PIN(sA[2][2][1]); SBAR(); }
  { MF_SA(sB[0][0], kf[2], 200); kf[0] = KRD(1, 1, 0); SBAR(); }
  { MF_SA(sB[1][0], kf[2], 232); DMA_V1(NT - 3, 1, 2); SBAR(); }
  { MF_SV(sB[2][0], kf[2], qf2[2]); sA[2][2][2] = exp_s(ffma_s(sA[2][2][2], csc, nmc[2])); PIN(sA[2][2][2]); SBAR(); }
  { MF_SA(sB[0][0], kf[3], 204); kf[1] = KRD(1, 1, 1); SBAR(); }
  { MF_SA(sB[1][0], kf[3], 236); DMA_V1(NT - 3, 1, 3); SBAR(); }
  { MF_SV(sB[2][0], kf[3], qf2[3]); sA[2][2][3] = exp_s(ffma_s(sA[2][2][3], csc, nmc[2])); PIN(sA[2][2][3]); SBAR(); }
  { MF_SA(sB[0][0], kf[4], 208); kf[2] = KRD(1, 1, 2); SBAR(); }
  { MF_SA(sB[1][0], kf[4], 240); DMA_V1(NT - 3, 1, 4); SBAR(); }
  { MF_SV(sB[2][0], kf[4], qf2[4]); sA[2][3][0] = exp_s(ffma_s(sA[2][3][0], csc, nmc[2])); PIN(sA[2][3][0]); SBAR(); }
  { MF_SA(sB[0][0], kf[5], 212); kf[3] = KRD(1, 1, 3); SBAR(); }
  { MF_SA(sB[1][0], kf[5], 244); DMA_V1(NT - 3, 1, 5); SBAR(); }
  { MF_SV(sB[2][0], kf[5], qf2[5]); sA[2][3][1] = exp_s(ffma_s(sA[2][3][1], csc, nmc[2])); PIN(sA[2][3][1]); SBAR(); }
  { MF_SA(sB[0][0], kf[6], 216); kf[4] = KRD(1, 1, 4); SBAR(); }
  { MF_SA(sB[1][0], kf[6], 248); DMA_V1(NT - 3, 1, 6); SBAR(); }
  { MF_SV(sB[2][0], kf[6], qf2[6]); sA[2][3][2] = exp_s(ffma_s(sA[2][3][2], csc, nmc[2])); PIN(sA[2][3][2]); SBAR(); }
  { MF_SA(sB[0][0], kf[7], 220); kf[5] = KRD(1, 1, 5); SBAR(); }
  { MF_SA(sB[1][0], kf[7], 252); DMA_V1(NT - 3, 1, 7); SBAR(); }
  { MF_SV(sB[2][0], kf[7], qf2[7]); sA[2][3][3] = exp_s(ffma_s(sA[2][3][3], csc, nmc[2])); PIN(sA[2][3][3]); SBAR(); }
  { MF_S0A(sB[0][1], kf[0], 192); kf[6] = KRD(1, 1, 6); SBAR(); }
  { MF_S0A(sB[1][1], kf[0], 224); l[0] = fadd_s(l[0], sA[0][0][0]); PIN(l[0]); l[0] = fadd_s(l[0], sA[0][0][1]); PIN(l[0]); SBAR(); }
  { MF_S0V(sB[2][1], kf[0], qf2[0]); pw[0][0][0] = cvtpk(sA[0][0][0], sA[0][0][1]); PIN(pw[0][0][0]); SBAR(); }
  { MF_SA(sB[0][1], kf[1], 196); kf[7] = KRD(1, 1, 7); SBAR(); }
  { MF_SA(sB[1][1], kf[1], 228); l[0] = fadd_s(l[0], sA[0][0][2]); PIN(l[0]); l[0] = fadd_s(l[0], sA[0][0][3]); PIN(l[0]); SBAR(); }
  { MF_SV(sB[2][1], kf[1], qf2[1]); pw[0][0][1] = cvtpk(sA[0][0][2], sA[0][0][3]); PIN(pw[0][0][1]); SBAR(); }
  { MF_SA(sB[0][1], kf[2], 200); kf[0] = KRD(1, 2, 0); SBAR(); }
  { MF_SA(sB[1][1], kf[2], 232); l[0] = fadd_s(l[0], sA[0][1][0]); PIN(l[0]); l[0] = fadd_s(l[0], sA[0][1][1]); PIN(l[0]); SBAR(); }
  { MF_SV(sB[2][1], kf[2], qf2[2]); pw[0][0][2] = cvtpk(sA[0][1][0], sA[0][1][1]); PIN(pw[0][0][2]); SBAR(); }
  { MF_SA(sB[0][1], kf[3], 204); kf[1] = KRD(1, 2, 1); SBAR(); }
  { MF_SA(sB[1][1], kf[3], 236); l[0] = fadd_s(l[0], sA[0][1][2]); PIN(l[0]); l[0] = fadd_s(l[0], sA[0][1][3]); PIN(l[0]); SBAR(); }
  { MF_SV(sB[2][1], kf[3], qf2[3]); pw[0][0][3] = cvtpk(sA[0][1][2], sA[0][1][3]); PIN(pw[0][0][3]); SBAR(); }
  { MF_SA(sB[0][1], kf[4], 208); kf[2] = KRD(1, 2, 2); SBAR(); }
  { MF_SA(sB[1][1], kf[4], 240); l[0] = fadd_s(l[0], sA[0][2][0]); PIN(l[0]); l[0] = fadd_s(l[0], sA[0][2][1]); PIN(l[0]); SBAR(); }
  { MF_SV(sB[2][1], kf[4], qf2[4]); pw[0][1][0] = cvtpk(sA[0][2][0], sA[0][2][1]); PIN(pw[0][1][0]); SBAR(); }
  { MF_SA(sB[0][1], kf[5], 212); kf[3] = KRD(1, 2, 3); SBAR(); }
  { MF_SA(sB[1][1], kf[5], 244); l[0] = fadd_s(l[0], sA[0][2][2]); PIN(l[0]); l[0] = fadd_s(l[0], sA[0][2][3]); PIN(l[0]); SBAR(); }
  { MF_SV(sB[2][1], kf[5], qf2[5]); pw[0][1][1] = cvtpk(sA[0][2][2], sA[0][2][3]); PIN(pw[0][1][1]); SBAR(); }
  { MF_SA(sB[0][1], kf[6], 216); kf[4] = KRD(1, 2, 4); SBAR(); }
  { MF_SA(sB[1][1], kf[6], 248); l[0] = fadd_s(l[0], sA[0][3][0]); PIN(l[0]); l[0] = fadd_s(l[0], sA[0][3][1]); PIN(l[0]); SBAR(); }
  { MF_SV(sB[2][1], kf[6], qf2[6]); pw[0][1][2] = cvtpk(sA[0][3][0], sA[0][3][1]); PIN(pw[0][1][2]); SBAR(); }
  { MF_SA(sB[0][1], kf[7], 220); kf[5] = KRD(1, 2, 5); SBAR(); }
  { MF_SA(sB[1][1], kf[7], 252); l[0] = fadd_s(l[0], sA[0][3][2]); PIN(l[0]); l[0] = fadd_s(l[0], sA[0][3][3]); PIN(l[0]); SBAR(); }
  { MF_SV(sB[2][1], kf[7], qf2[7]); pw[0][1][3] = cvtpk(sA[0][3][2], sA[0][3][3]); PIN(pw[0][1][3]); SBAR(); }
  { MF_S0A(sB[0][2], kf[0], 192); kf[6] = KRD(1, 2, 6); SBAR(); }
  { MF_S0A(sB[1][2], kf[0], 224); l[1] = fadd_s(l[1], sA[1][0][0]); PIN(l[1]); l[1] = fadd_s(l[1], sA[1][0][1]); PIN(l[1]); SBAR(); }
  { MF_S0V(sB[2][2], kf[0], qf2[0]); pw[1][0][0] = cvtpk(sA[1][0][0], sA[1][0][1]); PIN(pw[1][0][0]); SBAR(); }
  { MF_SA(sB[0][2], kf[1], 196); kf[7] = KRD(1, 2, 7); SBAR(); }
  { MF_SA(sB[1][2], kf[1], 228); l[1] = fadd_s(l[1], sA[1][0][2]); PIN(l[1]); l[1] = fadd_s(l[1], sA[1][0][3]); PIN(l[1]); SBAR(); }
  { MF_SV(sB[2][2], kf[1], qf2[1]); pw[1][0][1] = cvtpk(sA[1][0][2], sA[1][0][3]); PIN(pw[1][0][1]); SBAR(); }
  { MF_SA(sB[0][2], kf[2], 200); kf[0] = KRD(1, 3, 0); SBAR(); }
  { MF_SA(sB[1][2], kf[2], 232); l[1] = fadd_s(l[1], sA[1][1][0]); PIN(l[1]); l[1] = fadd_s(l[1], sA[1][1][1]); PIN(l[1]); SBAR(); }
  { MF_SV(sB[2][2], kf[2], qf2[2]); pw[1][0][2] = cvtpk(sA[1][1][0], sA[1][1][1]); PIN(pw[1][0][2]); SBAR(); }
  { MF_SA(sB[0][2], kf[3], 204); kf[1] = KRD(1, 3, 1); SBAR(); }
  { MF_SA(sB[1][2], kf[3], 236); l[1] = fadd_s(l[1], sA[1][1][2]); PIN(l[1]); l[1] = fadd_s(l[1], sA[1][1][3]); PIN(l[1]); SBAR(); }
  { MF_SV(sB[2][2], kf[3], qf2[3]); pw[1][0][3] = cvtpk(sA[1][1][2], sA[1][1][3]); PIN(pw[1][0][3]); SBAR(); }
  { MF_SA(sB[0][2], kf[4], 208); kf[2] = KRD(1, 3, 2); SBAR(); }
  { MF_SA(sB[1][2], kf[4], 240); l[1] = fadd_s(l[1], sA[1][2][0]); PIN(l[1]); l[1] = fadd_s(l[1], sA[1][2][1]); PIN(l[1]); SBAR(); }
  { MF_SV(sB[2][2], kf[4], qf2[4]); pw[1][1][0] = cvtpk(sA[1][2][0], sA[1][2][1]); PIN(pw[1][1][0]); SBAR(); }
  { MF_SA(sB[0][2], kf[5], 212); kf[3] = KRD(1, 3, 3); SBAR(); }
  { MF_SA(sB[1][2], kf[5], 244); l[1] = fadd_s(l[1], sA[1][2][2]); PIN(l[1]); l[1] = fadd_s(l[1], sA[1][2][3]); PIN(l[1]); SBAR(); }
  { MF_SV(sB[2][2], kf[5], qf2[5]); pw[1][1][1] = cvtpk(sA[1][2][2], sA[1][2][3]); PIN(pw[1][1][1]); SBAR(); }
  { MF_SA(sB[0][2], kf[6], 216); kf[4] = KRD(1, 3, 4); SBAR(); }
  { MF_SA(sB[1][2], kf[6], 248); l[1] = fadd_s(l[1], sA[1][3][0]); PIN(l[1]); l[1] = fadd_s(l[1], sA[1][3][1]); PIN(l[1]); SBAR(); }
  { MF_SV(sB[2][2], kf[6], qf2[6]); pw[1][1][2] = cvtpk(sA[1][3][0], sA[1][3][1]); PIN(pw[1][1][2]); SBAR(); }
  { MF_SA(sB[0][2], kf[7], 220); kf[5] = KRD(1, 3, 5); SBAR(); }
  { MF_SA(sB[1][2], kf[7], 252); l[1] = fadd_s(l[1], sA[1][3][2]); PIN(l[1]); l[1] = fadd_s(l[1], sA[1][3][3]); PIN(l[1]); SBAR(); }
  { MF_SV(sB[2][2], kf[7], qf2[7]); pw[1][1][3] = cvtpk(sA[1][3][2], sA[1][3][3]); PIN(pw[1][1][3]); SBAR(); }
  { MF_S0A(sB[0][3], kf[0], 192); kf[6] = KRD(1, 3, 6); SBAR(); }
  { MF_S0A(sB[1][3], kf[0], 224); l[2] = fadd_s(l[2], sA[2][0][0]); PIN(l[2]); l[2] = fadd_s(l[2], sA[2][0][1]); PIN(l[2]); SBAR(); }
  { MF_S0V(sB[2][3], kf[0], qf2[0]); pw[2][0][0] = cvtpk(sA[2][0][0], sA[2][0][1]); PIN(pw[2][0][0]); SBAR(); }
  { MF_SA(sB[0][3], kf[1], 196); kf[7] = KRD(1, 3, 7); SBAR(); }
  { MF_SA(sB[1][3], kf[1], 228); l[2] = fadd_s(l[2], sA[2][0][2]); PIN(l[2]); l[2] = fadd_s(l[2], sA[2][0][3]); PIN(l[2]); SBAR(); }
  { MF_SV(sB[2][3], kf[1], qf2[1]); pw[2][0][1] = cvtpk(sA[2][0][2], sA[2][0][3]); PIN(pw[2][0][1]); SBAR(); }
  { MF_SA(sB[0][3], kf[2], 200); vf[0] = VRD(0, 0, 0); SBAR(); }
  { MF_SA(sB[1][3], kf[2], 232); l[2] = fadd_s(l[2], sA[2][1][0]); PIN(l[2]); l[2] = fadd_s(l[2], sA[2][1][1]); PIN(l[2]); SBAR(); }
  { MF_SV(sB[2][3], kf[2], qf2[2]); pw[2][0][2] = cvtpk(sA[2][1][0], sA[2][1][1]); PIN(pw[2][0][2]); SBAR(); }
  { MF_SA(sB[0][3], kf[3], 204); vf[1] = VRD(0, 0, 1); SBAR(); }
  { MF_SA(sB[1][3], kf[3], 236); l[2] = fadd_s(l[2], sA[2][1][2]); PIN(l[2]); l[2] = fadd_s(l[2], sA[2][1][3]); PIN(l[2]); SBAR(); }
  { MF_SV(sB[2][3], kf[3], qf2[3]); pw[2][0][3] = cvtpk(sA[2][1][2], sA[2][1][3]); PIN(pw[2][0][3]); SBAR(); }
  { MF_SA(sB[0][3], kf[4], 208); vf[2] = VRD(0, 0, 2); SBAR(); }
  { MF_SA(sB[1][3], kf[4], 240); l[2] = fadd_s(l[2], sA[2][2][0]); PIN(l[2]); l[2] = fadd_s(l[2], sA[2][2][1]); PIN(l[2]); SBAR(); }
  { MF_SV(sB[2][3], kf[4], qf2[4]); pw[2][1][0] = cvtpk(sA[2][2][0], sA[2][2][1]); PIN(pw[2][1][0]); SBAR(); }
  { MF_SA(sB[0][3], kf[5], 212); vf[3] = VRD(0, 0, 3); SBAR(); }
  { MF_SA(sB[1][3], kf[5], 244); l[2] = fadd_s(l[2], sA[2][2][2]); PIN(l[2]); l[2] = fadd_s(l[2], sA[2][2][3]); PIN(l[2]); SBAR(); }
  { MF_SV(sB[2][3], kf[5], qf2[5]); pw[2][1][1] = cvtpk(sA[2][2][2], sA[2][2][3]); PIN(pw[2][1][1]); SBAR(); }
  { MF_SA(sB[0][3], kf[6], 216); vf[4] = VRD(0, 0, 4); SBAR(); }
  { MF_SA(sB[1][3], kf[6], 248); l[2] = fadd_s(l[2], sA[2][3][0]); PIN(l[2]); l[2] = fadd_s(l[2], sA[2][3][1]); PIN(l[2]); SBAR(); }
  { MF_SV(sB[2][3], kf[6], qf2[6]); pw[2][1][2] = cvtpk(sA[2][3][0], sA[2][3][1]); PIN(pw[2][1][2]); SBAR(); }
  { MF_SA(sB[0][3], kf[7], 220); vf[5] = VRD(0, 0, 5); SBAR(); }
  { MF_SA(sB[1][3], kf[7], 252); l[2] = fadd_s(l[2], sA[2][3][2]); PIN(l[2]); l[2] = fadd_s(l[2], sA[2][3][3]); PIN(l[2]); SBAR(); }
  { MF_SV(sB[2][3], kf[7], qf2[7]); pw[2][1][3] = cvtpk(sA[2][3][2], sA[2][3][3]); PIN(pw[2][1][3]); SBAR(); }
  _Pragma("unroll") for (int sb = 0; sb < 3; ++sb) { pf[sb][0] = __builtin_bit_cast(bf16x8, pw[sb][0]); pf[sb][1] = __builtin_bit_cast(bf16x8, pw[sb][1]); PIN(pf[sb][0]); PIN(pf[sb][1]); }
  asm volatile("s_nop 1" ::: "memory"); SBAR();
  asm volatile("s_waitcnt vmcnt(16)\n\ts_barrier" ::: "memory");
  { MF_O(0, vf[0], pf[0][0]); vf[6] = VRD(0, 0, 6); SBAR(); }
  { MF_O(64, vf[0], pf[1][0]); mx[0] = max2f(sB[0][0][0], sB[0][0][1]); PIN(mx[0]); SBAR(); }
  { MF_O(128, vf[0], pf[2][0]); mx[1] = max2f(sB[1][0][0], sB[1][0][1]); PIN(mx[1]); SBAR(); }
  { MF_O(4, vf[1], pf[0][0]); DMA_K1(NT - 1, 1, 0); vf[7] = VRD(0, 0, 7); SBAR(); }
  { MF_O(68, vf[1], pf[1][0]); mx[2] = max2f(sB[2][0][0], sB[2][0][1]); PIN(mx[2]); SBAR(); }
  { MF_O(132, vf[1], pf[2][0]); mx[0] = max3f(mx[0], sB[0][0][2], sB[0][0][3]); PIN(mx[0]); SBAR(); }
  { MF_O(8, vf[2], pf[0][0]); vf[0] = VRD(0, 0, 8); SBAR(); }
  { MF_O(72, vf[2], pf[1][0]); mx[1] = max3f(mx[1], sB[1][0][2], sB[1][0][3]); PIN(mx[1]); SBAR(); }
  { MF_O(136, vf[2], pf[2][0]); mx[2] = max3f(mx[2], sB[2][0][2], sB[2][0][3]); PIN(mx[2]); SBAR(); }
  { MF_O(12, vf[3], pf[0][0]); DMA_K1(NT - 1, 1, 1); vf[1] = VRD(0, 0, 9); SBAR(); }
  { MF_O(76, vf[3], pf[1][0]); mx[0] = max3f(mx[0], sB[0][1][0], sB[0][1][1]); PIN(mx[0]); SBAR(); }
  { MF_O(140, vf[3], pf[2][0]); mx[1] = max3f(mx[1], sB[1][1][0], sB[1][1][1]); PIN(mx[1]); SBAR(); }
  { MF_O(16, vf[4], pf[0][0]); vf[2] = VRD(0, 0, 10); SBAR(); }
  { MF_O(80, vf[4], pf[1][0]); mx[2] = max3f(mx[2], sB[2][1][0], sB[2][1][1]); PIN(mx[2]); SBAR(); }
  { MF_O(144, vf[4], pf[2][0]); mx[0] = max3f(mx[0], sB[0][1][2], sB[0][1][3]); PIN(mx[0]); SBAR(); }
  { MF_O(20, vf[5], pf[0][0]); DMA_K1(NT - 1, 1, 2); vf[3] = VRD(0, 0, 11); SBAR(); }
  { MF_O(84, vf[5], pf[1][0]); mx[1] = max3f(mx[1], sB[1][1][2], sB[1][1][3]); PIN(mx[1]); SBAR(); }
  { MF_O(148, vf[5], pf[2][0]); mx[2] = max3f(mx[2], sB[2][1][2], sB[2][1][3]); PIN(mx[2]); SBAR(); }
  { MF_O(24, vf[6], pf[0][0]); vf[4] = VRD(0, 0, 12); SBAR(); }
  { MF_O(88, vf[6], pf[1][0]); mx[0] = max3f(mx[0], sB[0][2][0], sB[0][2][1]); PIN(mx[0]); SBAR(); }
  { MF_O(152, vf[6], pf[2][0]); mx[1] = max3f(mx[1], sB[1][2][0], sB[1][2][1]); PIN(mx[1]); SBAR(); }
  { MF_O(28, vf[7], pf[0][0]); DMA_K1(NT - 1, 1, 3); vf[5] = VRD(0, 0, 13); SBAR(); }
  { MF_O(92, vf[7], pf[1][0]); mx[2] = max3f(mx[2], sB[2][2][0], sB[2][2][1]); PIN(mx[2]); SBAR(); }
  { MF_O(156, vf[7], pf[2][0]); mx[0] = max3f(mx[0], sB[0][2][2], sB[0][2][3]); PIN(mx[0]); SBAR(); }
  { MF_O(32, vf[0], pf[0][0]); vf[6] = VRD(0, 0, 14); SBAR(); }
  { MF_O(96, vf[0], pf[1][0]); mx[1] = max3f(mx[1], sB[1][2][2], sB[1][2][3]); PIN(mx[1]); SBAR(); }
  { MF_O(160, vf[0], pf[2][0]); mx[2] = max3f(mx[2], sB[2][2][2], sB[2][2][3]); PIN(mx[2]); SBAR(); }
  { MF_O(36, vf[1], pf[0][0]); DMA_K1(NT - 1, 1, 4); vf[7] = VRD(0, 0, 15); SBAR(); }
  { MF_O(100, vf[1], pf[1][0]); mx[0] = max3f(mx[0], sB[0][3][0], sB[0][3][1]); PIN(mx[0]); SBAR(); }
  { MF_O(164, vf[1], pf[2][0]); mx[1] = max3f(mx[1], sB[1][3][0], sB[1][3][1]); PIN(mx[1]); SBAR(); }
  { MF_O(40, vf[2], pf[0][0]); vf[0] = VRD(0, 1, 0); SBAR(); }
  { MF_O(104, vf[2], pf[1][0]); mx[2] = max3f(mx[2], sB[2][3][0], sB[2][3][1]); PIN(mx[2]); SBAR(); }
  { MF_O(168, vf[2], pf[2][0]); mx[0] = max3f(mx[0], sB[0][3][2], sB[0][3][3]); PIN(mx[0]); SBAR(); }
  { MF_O(44, vf[3], pf[0][0]); DMA_K1(NT - 1, 1, 5); vf[1] = VRD(0, 1, 1); SBAR(); }
  { MF_O(108, vf[3], pf[1][0]); mx[1] = max3f(mx[1], sB[1][3][2], sB[1][3][3]); PIN(mx[1]); SBAR(); }
  { MF_O(172, vf[3], pf[2][0]); mx[2] = max3f(mx[2], sB[2][3][2], sB[2][3][3]); PIN(mx[2]); SBAR(); }
  { MF_O(48, vf[4], pf[0][0]); vf[2] = VRD(0, 1, 2); SBAR(); }
  { MF_O(112, vf[4], pf[1][0]); mx[0] = xmax16(mx[0]); PIN(mx[0]); SBAR(); }
  { MF_O(176, vf[4], pf[2][0]); mx[1] = xmax16(mx[1]); PIN(mx[1]); SBAR(); }
  { MF_O(52, vf[5], pf[0][0]); DMA_K1(NT - 1, 1, 6); vf[3] = VRD(0, 1, 3); SBAR(); }
  { MF_O(116, vf[5], pf[1][0]); mx[2] = xmax16(mx[2]); PIN(mx[2]); SBAR(); }
  { MF_O(180, vf[5], pf[2][0]); mx[0] = xmax32(mx[0]); PIN(mx[0]); SBAR(); }
  { MF_O(56, vf[6], pf[0][0]); vf[4] = VRD(0, 1, 4); SBAR(); }
  { MF_O(120, vf[6], pf[1][0]); mx[1] = xmax32(mx[1]); PIN(mx[1]); SBAR(); }
  { MF_O(184, vf[6], pf[2][0]); mx[2] = xmax32(mx[2]); PIN(mx[2]); SBAR(); }
  { MF_O(60, vf[7], pf[0][0]); DMA_K1(NT - 1, 1, 7); vf[5] = VRD(0, 1, 5); SBAR(); }
  {
    const bool grow = (mx[0] - m[0] > THR) | (mx[1] - m[1] > THR) | (mx[2] - m[2] > THR);
    if (__builtin_expect(__any((int)grow), 0)) {
#pragma unroll
      for (int sb = 0; sb < 3; ++sb) { const float mn = fmaxf(m[sb], mx[sb]);
        const float al = __builtin_amdgcn_exp2f((m[sb] - mn) * CSC); m[sb] = mn; nmc[sb] = -mn * CSC; l[sb] *= al; alv[sb] = al; }
      pend = 1;
    }
    SBAR();
  }
  { MF_O(124, vf[7], pf[1][0]); sB[0][0][0] = exp_s(ffma_s(sB[0][0][0], csc, nmc[0])); PIN(sB[0][0][0]); SBAR(); }
  { MF_O(188, vf[7], pf[2][0]); sB[0][0][1] = exp_s(ffma_s(sB[0][0][1], csc, nmc[0])); PIN(sB[0][0][1]); SBAR(); }
  { MF_O(0, vf[0], pf[0][1]); vf[6] = VRD(0, 1, 6); sB[0][0][2] = exp_s(ffma_s(sB[0][0][2], csc, nmc[0])); PIN(sB[0][0][2]); SBAR(); }
  { MF_O(64, vf[0], pf[1][1]); sB[0][0][3] = exp_s(ffma_s(sB[0][0][3], csc, nmc[0])); PIN(sB[0][0][3]); SBAR(); }
  { MF_O(128, vf[0], pf[2][1]); sB[0][1][0] = exp_s(ffma_s(sB[0][1][0], csc, nmc[0])); PIN(sB[0][1][0]); SBAR(); }
  { MF_O(4, vf[1], pf[0][1]); vf[7] = VRD(0, 1, 7); sB[0][1][1] = exp_s(ffma_s(sB[0][1][1], csc, nmc[0])); PIN(sB[0][1][1]); SBAR(); }
  { MF_O(68, vf[1], pf[1][1]); sB[0][1][2] = exp_s(ffma_s(sB[0][1][2], csc, nmc[0])); PIN(sB[0][1][2]); SBAR(); }
  { MF_O(132, vf[1], pf[2][1]); sB[0][1][3] = exp_s(ffma_s(sB[0][1][3], csc, nmc[0])); PIN(sB[0][1][3]); SBAR(); }
  { MF_O(8, vf[2], pf[0][1]); vf[0] = VRD(0, 1, 8); sB[0][2][0] = exp_s(ffma_s(sB[0][2][0], csc, nmc[0])); PIN(sB[0][2][0]); SBAR(); }
  { MF_O(72, vf[2], pf[1][1]); sB[0][2][1] = exp_s(ffma_s(sB[0][2][1], csc, nmc[0])); PIN(sB[0][2][1]); SBAR(); }
  { MF_O(136, vf[2], pf[2][1]); sB[0][2][2] = exp_s(ffma_s(sB[0][2][2], csc, nmc[0])); PIN(sB[0][2][2]); SBAR(); }
  { MF_O(12, vf[3], pf[0][1]); vf[1] = VRD(0, 1, 9); sB[0][2][3] = exp_s(ffma_s(sB[0][2][3], csc, nmc[0])); PIN(sB[0][2][3]); SBAR(); }
  { MF_O(76, vf[3], pf[1][1]); sB[0][3][0] = exp_s(ffma_s(sB[0][3][0], csc, nmc[0])); PIN(sB[0][3][0]); SBAR(); }
  { MF_O(140, vf[3], pf[2][1]); sB[0][3][1] = exp_s(ffma_s(sB[0][3][1], csc, nmc[0])); PIN(sB[0][3][1]); SBAR(); }
  { MF_O(16, vf[4], pf[0][1]); vf[2] = VRD(0, 1, 10); sB[0][3][2] = exp_s(ffma_s(sB[0][3][2], csc, nmc[0])); PIN(sB[0][3][2]); SBAR(); }
  { MF_O(80, vf[4], pf[1][1]); sB[0][3][3] = exp_s(ffma_s(sB[0][3][3], csc, nmc[0])); PIN(sB[0][3][3]); SBAR(); }
  { MF_O(144, vf[4], pf[2][1]); sB[1][0][0] = exp_s(ffma_s(sB[1][0][0], csc, nmc[1])); PIN(sB[1][0][0]); SBAR(); }
  { MF_O(20, vf[5], pf[0][1]); vf[3] = VRD(0, 1, 11); sB[1][0][1] = exp_s(ffma_s(sB[1][0][1], csc, nmc[1])); PIN(sB[1][0][1]); SBAR(); }
  { MF_O(84, vf[5], pf[1][1]); sB[1][0][2] = exp_s(ffma_s(sB[1][0][2], csc, nmc[1])); PIN(sB[1][0][2]); SBAR(); }
  { MF_O(148, vf[5], pf[2][1]); sB[1][0][3] = exp_s(ffma_s(sB[1][0][3], csc, nmc[1])); PIN(sB[1][0][3]); SBAR(); }
  { MF_O(24, vf[6], pf[0][1]); vf[4] = VRD(0, 1, 12); SBAR(); }
  { MF_O(88, vf[6], pf[1][1]); sB[1][1][0] = exp_s(ffma_s(sB[1][1][0], csc, nmc[1])); PIN(sB[1][1][0]); SBAR(); }
  { MF_O(152, vf[6], pf[2][1]); sB[1][1][1] = exp_s(ffma_s(sB[1][1][1], csc, nmc[1])); PIN(sB[1][1][1]); SBAR(); }
  { MF_O(28, vf[7], pf[0][1]); vf[5] = VRD(0, 1, 13); SBAR(); }
  { MF_O(92, vf[7], pf[1][1]); sB[1][1][2] = exp_s(ffma_s(sB[1][1][2], csc, nmc[1])); PIN(sB[1][1][2]); SBAR(); }
  { MF_O(156, vf[7], pf[2][1]); sB[1][1][3] = exp_s(ffma_s(sB[1][1][3], csc, nmc[1])); PIN(sB[1][1][3]); SBAR(); }
  { MF_O(32, vf[0], pf[0][1]); vf[6] = VRD(0, 1, 14); SBAR(); }
  { MF_O(96, vf[0], pf[1][1]); sB[1][2][0] = exp_s(ffma_s(sB[1][2][0], csc, nmc[1])); PIN(sB[1][2][0]); SBAR(); }
  { MF_O(160, vf[0], pf[2][1]); sB[1][2][1] = exp_s(ffma_s(sB[1][2][1], csc, nmc[1])); PIN(sB[1][2][1]); SBAR(); }
  { MF_O(36, vf[1], pf[0][1]); vf[7] = VRD(0, 1, 15); SBAR(); }
  { MF_O(100, vf[1], pf[1][1]); sB[1][2][2] = exp_s(ffma_s(sB[1][2][2], csc, nmc[1])); PIN(sB[1][2][2]); SBAR(); }
  { MF_O(164, vf[1], pf[2][1]); sB[1][2][3] = exp_s(ffma_s(sB[1][2][3], csc, nmc[1])); PIN(sB[1][2][3]); SBAR(); }
  { MF_O(40, vf[2], pf[0][1]);  SBAR(); }
  { MF_O(104, vf[2], pf[1][1]); sB[1][3][0] = exp_s(ffma_s(sB[1][3][0], csc, nmc[1])); PIN(sB[1][3][0]); SBAR(); }
  { MF_O(168, vf[2], pf[2][1]); sB[1][3][1] = exp_s(ffma_s(sB[1][3][1], csc, nmc[1])); PIN(sB[1][3][1]); SBAR(); }
  { MF_O(44, vf[3], pf[0][1]);  SBAR(); }
  { MF_O(108, vf[3], pf[1][1]); sB[1][3][2] = exp_s(ffma_s(sB[1][3][2], csc, nmc[1])); PIN(sB[1][3][2]); SBAR(); }
  { MF_O(172, vf[3], pf[2][1]); sB[1][3][3] = exp_s(ffma_s(sB[1][3][3], csc, nmc[1])); PIN(sB[1][3][3]); SBAR(); }
  { MF_O(48, vf[4], pf[0][1]);  SBAR(); }
  { MF_O(112, vf[4], pf[1][1]); sB[2][0][0] = exp_s(ffma_s(sB[2][0][0], csc, nmc[2])); PIN(sB[2][0][0]); SBAR(); }
  { MF_O(176, vf[4], pf[2][1]); sB[2][0][1] = exp_s(ffma_s(sB[2][0][1], csc, nmc[2])); PIN(sB[2][0][1]); SBAR(); }
  { MF_O(52, vf[5], pf[0][1]);  SBAR(); }
  { MF_O(116, vf[5], pf[1][1]); sB[2][0][2] = exp_s(ffma_s(sB[2][0][2], csc, nmc[2])); PIN(sB[2][0][2]); SBAR(); }
  { MF_O(180, vf[5], pf[2][1]); sB[2][0][3] = exp_s(ffma_s(sB[2][0][3], csc, nmc[2])); PIN(sB[2][0][3]); SBAR(); }
  { MF_O(56, vf[6], pf[0][1]);  SBAR(); }
  { MF_O(120, vf[6], pf[1][1]); sB[2][1][0] = exp_s(ffma_s(sB[2][1][0], csc, nmc[2])); PIN(sB[2][1][0]); SBAR(); }
  { MF_O(184, vf[6], pf[2][1]); sB[2][1][1] = exp_s(ffma_s(sB[2][1][1], csc, nmc[2])); PIN(sB[2][1][1]); SBAR(); }
  { MF_O(60, vf[7], pf[0][1]);  SBAR(); }
  { MF_O(124, vf[7], pf[1][1]); sB[2][1][2] = exp_s(ffma_s(sB[2][1][2], csc, nmc[2])); PIN(sB[2][1][2]); SBAR(); }
  { MF_O(188, vf[7], pf[2][1]); sB[2][1][3] = exp_s(ffma_s(sB[2][1][3], csc, nmc[2])); PIN(sB[2][1][3]); SBAR(); }
  if (__builtin_expect(pend, 0)) {
    asm volatile("s_nop 7\n\ts_nop 7" ::: "memory");
      { float t0, t1; asm volatile("v_accvgpr_read_b32 %0, a0\n\tv_mul_f32 %0, %2, %0\n\tv_accvgpr_write_b32 a0, %0\n\t" "v_accvgpr_read_b32 %1, a1\n\tv_mul_f32 %1, %2, %1\n\tv_accvgpr_write_b32 a1, %1\n\t" "v_accvgpr_read_b32 %0, a2\n\tv_mul_f32 %0, %2, %0\n\tv_accvgpr_write_b32 a2, %0\n\t" "v_accvgpr_read_b32 %1, a3\n\tv_mul_f32 %1, %2, %1\n\tv_accvgpr_write_b32 a3, %1\n\t" "v_accvgpr_read_b32 %0, a4\n\tv_mul_f32 %0, %2, %0\n\tv_accvgpr_write_b32 a4, %0\n\t" "v_accvgpr_read_b32 %1, a5\n\tv_mul_f32 %1, %2, %1\n\tv_accvgpr_write_b32 a5, %1\n\t" "v_accvgpr_read_b32 %0, a6\n\tv_mul_f32 %0, %2, %0\n\tv_accvgpr_write_b32 a6, %0\n\t" "v_accvgpr_read_b32 %1, a7\n\tv_mul_f32 %1, %2, %1\n\tv_accvgpr_write_b32 a7, %1\n\t" "v_accvgpr_read_b32 %0, a8\n\tv_mul_f32 %0, %2, %0\n\tv_accvgpr_write_b32 a8, %0\n\t" "v_accvgpr_read_b32 %1, a9\n\tv_mul_f32 %1, %2, %1\n\tv_accvgpr_write_b32 a9, %1\n\t" "v_accvgpr_read_b32 %0, a10\n\tv_mul_f32 %0, %2, %0\n\tv_accvgpr_write_b32 a10, %0\n\t" "v_accvgpr_read_b32 %1, a11\n\tv_mul_f32 %1, %2, %1\n\tv_accvgpr_write_b32 a11, %1\n\t" "v_accvgpr_read_b32 %0, a12\n\tv_mul_f32 %0, %2, %0\n\tv_accvgpr_write_b32 a12, %0\n\t" "v_accvgpr_read_b32 %1, a13\n\tv_mul_f32 %1, %2, %1\n\tv_accvgpr_write_b32 a13, %1\n\t" "v_accvgpr_read_b32 %0, a14\n\tv_mul_f32 %0, %2, %0\n\tv_accvgpr_write_b32 a14, %0\n\t" "v_accvgpr_read_b32 %1, a15\n\tv_mul_f32 %1, %2, %1\n\tv_accvgpr_write_b32 a15, %1\n\t" "v_accvgpr_read_b32 %0, a16\n\tv_mul_f32 %0, %2, %0\n\tv_accvgpr_write_b32 a16, %0\n\t" "v_accvgpr_read_b32 %1, a17\n\tv_mul_f32 %1, %2, %1\n\tv_accvgpr_write_b32 a17, %1\n\t" "v_accvgpr_read_b32 %0, a18\n\tv_mul_f32 %0, %2, %0\n\tv_accvgpr_write_b32 a18, %0\n\t" "v_accvgpr_read_b32 %1, a19\n\tv_mul_f32 %1, %2, %1\n\tv_accvgpr_write_b32 a19, %1\n\t" "v_accvgpr_read_b32 %0, a20\n\tv_mul_f32 %0, %2, %0\n\tv_accvgpr_write_b32 a20, %0\n\t" "v_accvgpr_read_b32 %1, a21\n\tv_mul_f32 %1, %2, %1\n\tv_accvgpr_write_b32 a21, %1\n\t" "v_accvgpr_read_b32 %0, a22\n\tv_mul_f32 %0, %2, %0\n\tv_accvgpr_write_b32 a22, %0\n\t" "v_accvgpr_read_b32 %1, a23\n\tv_mul_f32 %1, %2, %1\n\tv_accvgpr_write_b32 a23, %1\n\t" "v_accvgpr_read_b32 %0, a24\n\tv_mul_f32 %0, %2, %0\n\tv_accvgpr_write_b32 a24, %0\n\t" "v_accvgpr_read_b32 %1, a25\n\tv_mul_f32 %1, %2, %1\n\tv_accvgpr_write_b32 a25, %1\n\t" "v_accvgpr_read_b32 %0, a26\n\tv_mul_f32 %0, %2, %0\n\tv_accvgpr_write_b32 a26, %0\n\t" "v_accvgpr_read_b32 %1, a27\n\tv_mul_f32 %1, %2, %1\n\tv_accvgpr_write_b32 a27, %1\n\t" "v_accvgpr_read_b32 %0, a28\n\tv_mul_f32 %0, %2, %0\n\tv_accvgpr_write_b32 a28, %0\n\t" "v_accvgpr_read_b32 %1, a29\n\tv_mul_f32 %1, %2, %1\n\tv_accvgpr_write_b32 a29, %1\n\t" "v_accvgpr_read_b32 %0, a30\n\tv_mul_f32 %0, %2, %0\n\tv_accvgpr_write_b32 a30, %0\n\t" "v_accvgpr_read_b32 %1, a31\n\tv_mul_f32 %1, %2, %1\n\tv_accvgpr_write_b32 a31, %1\n\t" "v_accvgpr_read_b32 %0, a32\n\tv_mul_f32 %0, %2, %0\n\tv_accvgpr_write_b32 a32, %0\n\t" "v_accvgpr_read_b32 %1, a33\n\tv_mul_f32 %1, %2, %1\n\tv_accvgpr_write_b32 a33, %1\n\t" "v_accvgpr_read_b32 %0, a34\n\tv_mul_f32 %0, %2, %0\n\tv_accvgpr_write_b32 a34, %0\n\t" "v_accvgpr_read_b32 %1, a35\n\tv_mul_f32 %1, %2, %1\n\tv_accvgpr_write_b32 a35, %1\n\t" "v_accvgpr_read_b32 %0, a36\n\tv_mul_f32 %0, %2, %0\n\tv_accvgpr_write_b32 a36, %0\n\t" "v_accvgpr_read_b32 %1, a37\n\tv_mul_f32 %1, %2, %1\n\tv_accvgpr_write_b32 a37, %1\n\t" "v_accvgpr_read_b32 %0, a38\n\tv_mul_f32 %0, %2, %0\n\tv_accvgpr_write_b32 a38, %0\n\t" "v_accvgpr_read_b32 %1, a39\n\tv_mul_f32 %1, %2, %1\n\tv_accvgpr_write_b32 a39, %1\n\t" "v_accvgpr_read_b32 %0, a40\n\tv_mul_f32 %0, %2, %0\n\tv_accvgpr_write_b32 a40, %0\n\t" "v_accvgpr_read_b32 %1, a41\n\tv_mul_f32 %1, %2, %1\n\tv_accvgpr_write_b32 a41, %1\n\t" "v_accvgpr_read_b32 %0, a42\n\tv_mul_f32 %0, %2, %0\n\tv_accvgpr_write_b32 a42, %0\n\t" "v_accvgpr_read_b32 %1, a43\n\tv_mul_f32 %1, %2, %1\n\tv_accvgpr_write_b32 a43, %1\n\t" "v_accvgpr_read_b32 %0, a44\n\tv_mul_f32 %0, %2, %0\n\tv_accvgpr_write_b32 a44, %0\n\t" "v_accvgpr_read_b32 %1, a45\n\tv_mul_f32 %1, %2, %1\n\tv_accvgpr_write_b32 a45, %1\n\t" "v_accvgpr_read_b32 %0, a46\n\tv_mul_f32 %0, %2, %0\n\tv_accvgpr_write_b32 a46, %0\n\t" "v_accvgpr_read_b32 %1, a47\n\tv_mul_f32 %1, %2, %1\n\tv_accvgpr_write_b32 a47, %1\n\t" "v_accvgpr_read_b32 %0, a48\n\tv_mul_f32 %0, %2, %0\n\tv_accvgpr_write_b32 a48, %0\n\t" "v_accvgpr_read_b32 %1, a49\n\tv_mul_f32 %1, %2, %1\n\tv_accvgpr_write_b32 a49, %1\n\t" "v_accvgpr_read_b32 %0, a50\n\tv_mul_f32 %0, %2, %0\n\tv_accvgpr_write_b32 a50, %0\n\t" "v_accvgpr_read_b32 %1, a51\n\tv_mul_f32 %1, %2, %1\n\tv_accvgpr_write_b32 a51, %1\n\t" "v_accvgpr_read_b32 %0, a52\n\tv_mul_f32 %0, %2, %0\n\tv_accvgpr_write_b32 a52, %0\n\t" "v_accvgpr_read_b32 %1, a53\n\tv_mul_f32 %1, %2, %1\n\tv_accvgpr_write_b32 a53, %1\n\t" "v_accvgpr_read_b32 %0, a54\n\tv_mul_f32 %0, %2, %0\n\tv_accvgpr_write_b32 a54, %0\n\t" "v_accvgpr_read_b32 %1, a55\n\tv_mul_f32 %1, %2, %1\n\tv_accvgpr_write_b32 a55, %1\n\t" "v_accvgpr_read_b32 %0, a56\n\tv_mul_f32 %0, %2, %0\n\tv_accvgpr_write_b32 a56, %0\n\t" "v_accvgpr_read_b32 %1, a57\n\tv_mul_f32 %1, %2, %1\n\tv_accvgpr_write_b32 a57, %1\n\t" "v_accvgpr_read_b32 %0, a58\n\tv_mul_f32 %0, %2, %0\n\tv_accvgpr_write_b32 a58, %0\n\t" "v_accvgpr_read_b32 %1, a59\n\tv_mul_f32 %1, %2, %1\n\tv_accvgpr_write_b32 a59, %1\n\t" "v_accvgpr_read_b32 %0, a60\n\tv_mul_f32 %0, %2, %0\n\tv_accvgpr_write_b32 a60, %0\n\t" "v_accvgpr_read_b32 %1, a61\n\tv_mul_f32 %1, %2, %1\n\tv_accvgpr_write_b32 a61, %1\n\t" "v_accvgpr_read_b32 %0, a62\n\tv_mul_f32 %0, %2, %0\n\tv_accvgpr_write_b32 a62, %0\n\t" "v_accvgpr_read_b32 %1, a63\n\tv_mul_f32 %1, %2, %1\n\tv_accvgpr_write_b32 a63, %1\n\t" : "=&v"(t0), "=&v"(t1) : "v"(alv[0]) : ACLOB); }
      { float t0, t1; asm volatile("v_accvgpr_read_b32 %0, a64\n\tv_mul_f32 %0, %2, %0\n\tv_accvgpr_write_b32 a64, %0\n\t" "v_accvgpr_read_b32 %1, a65\n\tv_mul_f32 %1, %2, %1\n\tv_accvgpr_write_b32 a65, %1\n\t" "v_accvgpr_read_b32 %0, a66\n\tv_mul_f32 %0, %2, %0\n\tv_accvgpr_write_b32 a66, %0\n\t" "v_accvgpr_read_b32 %1, a67\n\tv_mul_f32 %1, %2, %1\n\tv_accvgpr_write_b32 a67, %1\n\t" "v_accvgpr_read_b32 %0, a68\n\tv_mul_f32 %0, %2, %0\n\tv_accvgpr_write_b32 a68, %0\n\t" "v_accvgpr_read_b32 %1, a69\n\tv_mul_f32 %1, %2, %1\n\tv_accvgpr_write_b32 a69, %1\n\t" "v_accvgpr_read_b32 %0, a70\n\tv_mul_f32 %0, %2, %0\n\tv_accvgpr_write_b32 a70, %0\n\t" "v_accvgpr_read_b32 %1, a71\n\tv_mul_f32 %1, %2, %1\n\tv_accvgpr_write_b32 a71, %1\n\t" "v_accvgpr_read_b32 %0, a72\n\tv_mul_f32 %0, %2, %0\n\tv_accvgpr_write_b32 a72, %0\n\t" "v_accvgpr_read_b32 %1, a73\n\tv_mul_f32 %1, %2, %1\n\tv_accvgpr_write_b32 a73, %1\n\t" "v_accvgpr_read_b32 %0, a74\n\tv_mul_f32 %0, %2, %0\n\tv_accvgpr_write_b32 a74, %0\n\t" "v_accvgpr_read_b32 %1, a75\n\tv_mul_f32 %1, %2, %1\n\tv_accvgpr_write_b32 a75, %1\n\t" "v_accvgpr_read_b32 %0, a76\n\tv_mul_f32 %0, %2, %0\n\tv_accvgpr_write_b32 a76, %0\n\t" "v_accvgpr_read_b32 %1, a77\n\tv_mul_f32 %1, %2, %1\n\tv_accvgpr_write_b32 a77, %1\n\t" "v_accvgpr_read_b32 %0, a78\n\tv_mul_f32 %0, %2, %0\n\tv_accvgpr_write_b32 a78, %0\n\t" "v_accvgpr_read_b32 %1, a79\n\tv_mul_f32 %1, %2, %1\n\tv_accvgpr_write_b32 a79, %1\n\t" "v_accvgpr_read_b32 %0, a80\n\tv_mul_f32 %0, %2, %0\n\tv_accvgpr_write_b32 a80, %0\n\t" "v_accvgpr_read_b32 %1, a81\n\tv_mul_f32 %1, %2, %1\n\tv_accvgpr_write_b32 a81, %1\n\t" "v_accvgpr_read_b32 %0, a82\n\tv_mul_f32 %0, %2, %0\n\tv_accvgpr_write_b32 a82, %0\n\t" "v_accvgpr_read_b32 %1, a83\n\tv_mul_f32 %1, %2, %1\n\tv_accvgpr_write_b32 a83, %1\n\t" "v_accvgpr_read_b32 %0, a84\n\tv_mul_f32 %0, %2, %0\n\tv_accvgpr_write_b32 a84, %0\n\t" "v_accvgpr_read_b32 %1, a85\n\tv_mul_f32 %1, %2, %1\n\tv_accvgpr_write_b32 a85, %1\n\t" "v_accvgpr_read_b32 %0, a86\n\tv_mul_f32 %0, %2, %0\n\tv_accvgpr_write_b32 a86, %0\n\t" "v_accvgpr_read_b32 %1, a87\n\tv_mul_f32 %1, %2, %1\n\tv_accvgpr_write_b32 a87, %1\n\t" "v_accvgpr_read_b32 %0, a88\n\tv_mul_f32 %0, %2, %0\n\tv_accvgpr_write_b32 a88, %0\n\t" "v_accvgpr_read_b32 %1, a89\n\tv_mul_f32 %1, %2, %1\n\tv_accvgpr_write_b32 a89, %1\n\t" "v_accvgpr_read_b32 %0, a90\n\tv_mul_f32 %0, %2, %0\n\tv_accvgpr_write_b32 a90, %0\n\t" "v_accvgpr_read_b32 %1, a91\n\tv_mul_f32 %1, %2, %1\n\tv_accvgpr_write_b32 a91, %1\n\t" "v_accvgpr_read_b32 %0, a92\n\tv_mul_f32 %0, %2, %0\n\tv_accvgpr_write_b32 a92, %0\n\t" "v_accvgpr_read_b32 %1, a93\n\tv_mul_f32 %1, %2, %1\n\tv_accvgpr_write_b32 a93, %1\n\t" "v_accvgpr_read_b32 %0, a94\n\tv_mul_f32 %0, %2, %0\n\tv_accvgpr_write_b32 a94, %0\n\t" "v_accvgpr_read_b32 %1, a95\n\tv_mul_f32 %1, %2, %1\n\tv_accvgpr_write_b32 a95, %1\n\t" "v_accvgpr_read_b32 %0, a96\n\tv_mul_f32 %0, %2, %0\n\tv_accvgpr_write_b32 a96, %0\n\t" "v_accvgpr_read_b32 %1, a97\n\tv_mul_f32 %1, %2, %1\n\tv_accvgpr_write_b32 a97, %1\n\t" "v_accvgpr_read_b32 %0, a98\n\tv_mul_f32 %0, %2, %0\n\tv_accvgpr_write_b32 a98, %0\n\t" "v_accvgpr_read_b32 %1, a99\n\tv_mul_f32 %1, %2, %1\n\tv_accvgpr_write_b32 a99, %1\n\t" "v_accvgpr_read_b32 %0, a100\n\tv_mul_f32 %0, %2, %0\n\tv_accvgpr_write_b32 a100, %0\n\t" "v_accvgpr_read_b32 %1, a101\n\tv_mul_f32 %1, %2, %1\n\tv_accvgpr_write_b32 a101, %1\n\t" "v_accvgpr_read_b32 %0, a102\n\tv_mul_f32 %0, %2, %0\n\tv_accvgpr_write_b32 a102, %0\n\t" "v_accvgpr_read_b32 %1, a103\n\tv_mul_f32 %1, %2, %1\n\tv_accvgpr_write_b32 a103, %1\n\t" "v_accvgpr_read_b32 %0, a104\n\tv_mul_f32 %0, %2, %0\n\tv_accvgpr_write_b32 a104, %0\n\t" "v_accvgpr_read_b32 %1, a105\n\tv_mul_f32 %1, %2, %1\n\tv_accvgpr_write_b32 a105, %1\n\t" "v_accvgpr_read_b32 %0, a106\n\tv_mul_f32 %0, %2, %0\n\tv_accvgpr_write_b32 a106, %0\n\t" "v_accvgpr_read_b32 %1, a107\n\tv_mul_f32 %1, %2, %1\n\tv_accvgpr_write_b32 a107, %1\n\t" "v_accvgpr_read_b32 %0, a108\n\tv_mul_f32 %0, %2, %0\n\tv_accvgpr_write_b32 a108, %0\n\t" "v_accvgpr_read_b32 %1, a109\n\tv_mul_f32 %1, %2, %1\n\tv_accvgpr_write_b32 a109, %1\n\t" "v_accvgpr_read_b32 %0, a110\n\tv_mul_f32 %0, %2, %0\n\tv_accvgpr_write_b32 a110, %0\n\t" "v_accvgpr_read_b32 %1, a111\n\tv_mul_f32 %1, %2, %1\n\tv_accvgpr_write_b32 a111, %1\n\t" "v_accvgpr_read_b32 %0, a112\n\tv_mul_f32 %0, %2, %0\n\tv_accvgpr_write_b32 a112, %0\n\t" "v_accvgpr_read_b32 %1, a113\n\tv_mul_f32 %1, %2, %1\n\tv_accvgpr_write_b32 a113, %1\n\t" "v_accvgpr_read_b32 %0, a114\n\tv_mul_f32 %0, %2, %0\n\tv_accvgpr_write_b32 a114, %0\n\t" "v_accvgpr_read_b32 %1, a115\n\tv_mul_f32 %1, %2, %1\n\tv_accvgpr_write_b32 a115, %1\n\t" "v_accvgpr_read_b32 %0, a116\n\tv_mul_f32 %0, %2, %0\n\tv_accvgpr_write_b32 a116, %0\n\t" "v_accvgpr_read_b32 %1, a117\n\tv_mul_f32 %1, %2, %1\n\tv_accvgpr_write_b32 a117, %1\n\t" "v_accvgpr_read_b32 %0, a118\n\tv_mul_f32 %0, %2, %0\n\tv_accvgpr_write_b32 a118, %0\n\t" "v_accvgpr_read_b32 %1, a119\n\tv_mul_f32 %1, %2, %1\n\tv_accvgpr_write_b32 a119, %1\n\t" "v_accvgpr_read_b32 %0, a120\n\tv_mul_f32 %0, %2, %0\n\tv_accvgpr_write_b32 a120, %0\n\t" "v_accvgpr_read_b32 %1, a121\n\tv_mul_f32 %1, %2, %1\n\tv_accvgpr_write_b32 a121, %1\n\t" "v_accvgpr_read_b32 %0, a122\n\tv_mul_f32 %0, %2, %0\n\tv_accvgpr_write_b32 a122, %0\n\t" "v_accvgpr_read_b32 %1, a123\n\tv_mul_f32 %1, %2, %1\n\tv_accvgpr_write_b32 a123, %1\n\t" "v_accvgpr_read_b32 %0, a124\n\tv_mul_f32 %0, %2, %0\n\tv_accvgpr_write_b32 a124, %0\n\t" "v_accvgpr_read_b32 %1, a125\n\tv_mul_f32 %1, %2, %1\n\tv_accvgpr_write_b32 a125, %1\n\t" "v_accvgpr_read_b32 %0, a126\n\tv_mul_f32 %0, %2, %0\n\tv_accvgpr_write_b32 a126, %0\n\t" "v_accvgpr_read_b32 %1, a127\n\tv_mul_f32 %1, %2, %1\n\tv_accvgpr_write_b32 a127, %1\n\t" : "=&v"(t0), "=&v"(t1) : "v"(alv[1]) : ACLOB); }
      { float t0, t1; asm volatile("v_accvgpr_read_b32 %0, a128\n\tv_mul_f32 %0, %2, %0\n\tv_accvgpr_write_b32 a128, %0\n\t" "v_accvgpr_read_b32 %1, a129\n\tv_mul_f32 %1, %2, %1\n\tv_accvgpr_write_b32 a129, %1\n\t" "v_accvgpr_read_b32 %0, a130\n\tv_mul_f32 %0, %2, %0\n\tv_accvgpr_write_b32 a130, %0\n\t" "v_accvgpr_read_b32 %1, a131\n\tv_mul_f32 %1, %2, %1\n\tv_accvgpr_write_b32 a131, %1\n\t" "v_accvgpr_read_b32 %0, a132\n\tv_mul_f32 %0, %2, %0\n\tv_accvgpr_write_b32 a132, %0\n\t" "v_accvgpr_read_b32 %1, a133\n\tv_mul_f32 %1, %2, %1\n\tv_accvgpr_write_b32 a133, %1\n\t" "v_accvgpr_read_b32 %0, a134\n\tv_mul_f32 %0, %2, %0\n\tv_accvgpr_write_b32 a134, %0\n\t" "v_accvgpr_read_b32 %1, a135\n\tv_mul_f32 %1, %2, %1\n\tv_accvgpr_write_b32 a135, %1\n\t" "v_accvgpr_read_b32 %0, a136\n\tv_mul_f32 %0, %2, %0\n\tv_accvgpr_write_b32 a136, %0\n\t" "v_accvgpr_read_b32 %1, a137\n\tv_mul_f32 %1, %2, %1\n\tv_accvgpr_write_b32 a137, %1\n\t" "v_accvgpr_read_b32 %0, a138\n\tv_mul_f32 %0, %2, %0\n\tv_accvgpr_write_b32 a138, %0\n\t" "v_accvgpr_read_b32 %1, a139\n\tv_mul_f32 %1, %2, %1\n\tv_accvgpr_write_b32 a139, %1\n\t" "v_accvgpr_read_b32 %0, a140\n\tv_mul_f32 %0, %2, %0\n\tv_accvgpr_write_b32 a140, %0\n\t" "v_accvgpr_read_b32 %1, a141\n\tv_mul_f32 %1, %2, %1\n\tv_accvgpr_write_b32 a141, %1\n\t" "v_accvgpr_read_b32 %0, a142\n\tv_mul_f32 %0, %2, %0\n\tv_accvgpr_write_b32 a142, %0\n\t" "v_accvgpr_read_b32 %1, a143\n\tv_mul_f32 %1, %2, %1\n\tv_accvgpr_write_b32 a143, %1\n\t" "v_accvgpr_read_b32 %0, a144\n\tv_mul_f32 %0, %2, %0\n\tv_accvgpr_write_b32 a144, %0\n\t" "v_accvgpr_read_b32 %1, a145\n\tv_mul_f32 %1, %2, %1\n\tv_accvgpr_write_b32 a145, %1\n\t" "v_accvgpr_read_b32 %0, a146\n\tv_mul_f32 %0, %2, %0\n\tv_accvgpr_write_b32 a146, %0\n\t" "v_accvgpr_read_b32 %1, a147\n\tv_mul_f32 %1, %2, %1\n\tv_accvgpr_write_b32 a147, %1\n\t" "v_accvgpr_read_b32 %0, a148\n\tv_mul_f32 %0, %2, %0\n\tv_accvgpr_write_b32 a148, %0\n\t" "v_accvgpr_read_b32 %1, a149\n\tv_mul_f32 %1, %2, %1\n\tv_accvgpr_write_b32 a149, %1\n\t" "v_accvgpr_read_b32 %0, a150\n\tv_mul_f32 %0, %2, %0\n\tv_accvgpr_write_b32 a150, %0\n\t" "v_accvgpr_read_b32 %1, a151\n\tv_mul_f32 %1, %2, %1\n\tv_accvgpr_write_b32 a151, %1\n\t" "v_accvgpr_read_b32 %0, a152\n\tv_mul_f32 %0, %2, %0\n\tv_accvgpr_write_b32 a152, %0\n\t" "v_accvgpr_read_b32 %1, a153\n\tv_mul_f32 %1, %2, %1\n\tv_accvgpr_write_b32 a153, %1\n\t" "v_accvgpr_read_b32 %0, a154\n\tv_mul_f32 %0, %2, %0\n\tv_accvgpr_write_b32 a154, %0\n\t" "v_accvgpr_read_b32 %1, a155\n\tv_mul_f32 %1, %2, %1\n\tv_accvgpr_write_b32 a155, %1\n\t" "v_accvgpr_read_b32 %0, a156\n\tv_mul_f32 %0, %2, %0\n\tv_accvgpr_write_b32 a156, %0\n\t" "v_accvgpr_read_b32 %1, a157\n\tv_mul_f32 %1, %2, %1\n\tv_accvgpr_write_b32 a157, %1\n\t" "v_accvgpr_read_b32 %0, a158\n\tv_mul_f32 %0, %2, %0\n\tv_accvgpr_write_b32 a158, %0\n\t" "v_accvgpr_read_b32 %1, a159\n\tv_mul_f32 %1, %2, %1\n\tv_accvgpr_write_b32 a159, %1\n\t" "v_accvgpr_read_b32 %0, a160\n\tv_mul_f32 %0, %2, %0\n\tv_accvgpr_write_b32 a160, %0\n\t" "v_accvgpr_read_b32 %1, a161\n\tv_mul_f32 %1, %2, %1\n\tv_accvgpr_write_b32 a161, %1\n\t" "v_accvgpr_read_b32 %0, a162\n\tv_mul_f32 %0, %2, %0\n\tv_accvgpr_write_b32 a162, %0\n\t" "v_accvgpr_read_b32 %1, a163\n\tv_mul_f32 %1, %2, %1\n\tv_accvgpr_write_b32 a163, %1\n\t" "v_accvgpr_read_b32 %0, a164\n\tv_mul_f32 %0, %2, %0\n\tv_accvgpr_write_b32 a164, %0\n\t" "v_accvgpr_read_b32 %1, a165\n\tv_mul_f32 %1, %2, %1\n\tv_accvgpr_write_b32 a165, %1\n\t" "v_accvgpr_read_b32 %0, a166\n\tv_mul_f32 %0, %2, %0\n\tv_accvgpr_write_b32 a166, %0\n\t" "v_accvgpr_read_b32 %1, a167\n\tv_mul_f32 %1, %2, %1\n\tv_accvgpr_write_b32 a167, %1\n\t" "v_accvgpr_read_b32 %0, a168\n\tv_mul_f32 %0, %2, %0\n\tv_accvgpr_write_b32 a168, %0\n\t" "v_accvgpr_read_b32 %1, a169\n\tv_mul_f32 %1, %2, %1\n\tv_accvgpr_write_b32 a169, %1\n\t" "v_accvgpr_read_b32 %0, a170\n\tv_mul_f32 %0, %2, %0\n\tv_accvgpr_write_b32 a170, %0\n\t" "v_accvgpr_read_b32 %1, a171\n\tv_mul_f32 %1, %2, %1\n\tv_accvgpr_write_b32 a171, %1\n\t" "v_accvgpr_read_b32 %0, a172\n\tv_mul_f32 %0, %2, %0\n\tv_accvgpr_write_b32 a172, %0\n\t" "v_accvgpr_read_b32 %1, a173\n\tv_mul_f32 %1, %2, %1\n\tv_accvgpr_write_b32 a173, %1\n\t" "v_accvgpr_read_b32 %0, a174\n\tv_mul_f32 %0, %2, %0\n\tv_accvgpr_write_b32 a174, %0\n\t" "v_accvgpr_read_b32 %1, a175\n\tv_mul_f32 %1, %2, %1\n\tv_accvgpr_write_b32 a175, %1\n\t" "v_accvgpr_read_b32 %0, a176\n\tv_mul_f32 %0, %2, %0\n\tv_accvgpr_write_b32 a176, %0\n\t" "v_accvgpr_read_b32 %1, a177\n\tv_mul_f32 %1, %2, %1\n\tv_accvgpr_write_b32 a177, %1\n\t" "v_accvgpr_read_b32 %0, a178\n\tv_mul_f32 %0, %2, %0\n\tv_accvgpr_write_b32 a178, %0\n\t" "v_accvgpr_read_b32 %1, a179\n\tv_mul_f32 %1, %2, %1\n\tv_accvgpr_write_b32 a179, %1\n\t" "v_accvgpr_read_b32 %0, a180\n\tv_mul_f32 %0, %2, %0\n\tv_accvgpr_write_b32 a180, %0\n\t" "v_accvgpr_read_b32 %1, a181\n\tv_mul_f32 %1, %2, %1\n\tv_accvgpr_write_b32 a181, %1\n\t" "v_accvgpr_read_b32 %0, a182\n\tv_mul_f32 %0, %2, %0\n\tv_accvgpr_write_b32 a182, %0\n\t" "v_accvgpr_read_b32 %1, a183\n\tv_mul_f32 %1, %2, %1\n\tv_accvgpr_write_b32 a183, %1\n\t" "v_accvgpr_read_b32 %0, a184\n\tv_mul_f32 %0, %2, %0\n\tv_accvgpr_write_b32 a184, %0\n\t" "v_accvgpr_read_b32 %1, a185\n\tv_mul_f32 %1, %2, %1\n\tv_accvgpr_write_b32 a185, %1\n\t" "v_accvgpr_read_b32 %0, a186\n\tv_mul_f32 %0, %2, %0\n\tv_accvgpr_write_b32 a186, %0\n\t" "v_accvgpr_read_b32 %1, a187\n\tv_mul_f32 %1, %2, %1\n\tv_accvgpr_write_b32 a187, %1\n\t" "v_accvgpr_read_b32 %0, a188\n\tv_mul_f32 %0, %2, %0\n\tv_accvgpr_write_b32 a188, %0\n\t" "v_accvgpr_read_b32 %1, a189\n\tv_mul_f32 %1, %2, %1\n\tv_accvgpr_write_b32 a189, %1\n\t" "v_accvgpr_read_b32 %0, a190\n\tv_mul_f32 %0, %2, %0\n\tv_accvgpr_write_b32 a190, %0\n\t" "v_accvgpr_read_b32 %1, a191\n\tv_mul_f32 %1, %2, %1\n\tv_accvgpr_write_b32 a191, %1\n\t" : "=&v"(t0), "=&v"(t1) : "v"(alv[2]) : ACLOB); }
    asm volatile("s_nop 1" ::: "memory"); pend = 0;
  }
  SBAR();
  asm volatile("s_waitcnt vmcnt(16)\n\ts_barrier" ::: "memory");
  kf[0] = KRD(0, 0, 0); kf[1] = KRD(0, 0, 1); kf[2] = KRD(0, 0, 2); kf[3] = KRD(0, 0, 3); kf[4] = KRD(0, 0, 4); kf[5] = KRD(0, 0, 5);
  { MF_S0A(sA[0][0], kf[0], 192); kf[6] = KRD(0, 0, 6); SBAR(); }
  { MF_S0A(sA[1][0], kf[0], 224); DMA_V1(NT - 2, 0, 0); SBAR(); }
  { MF_S0V(sA[2][0], kf[0], qf2[0]); sB[2][2][0] = exp_s(ffma_s(sB[2][2][0], csc, nmc[2])); PIN(sB[2][2][0]); SBAR(); }
  { MF_SA(sA[0][0], kf[1], 196); kf[7] = KRD(0, 0, 7); SBAR(); }
  { MF_SA(sA[1][0], kf[1], 228); DMA_V1(NT - 2, 0, 1); SBAR(); }
  { MF_SV(sA[2][0], kf[1], qf2[1]); sB[2][2][1] = exp_s(ffma_s(sB[2][2][1], csc, nmc[2])); PIN(sB[2][2][1]); SBAR(); }
  { MF_SA(sA[0][0], kf[2], 200); kf[0] = KRD(0, 1, 0); SBAR(); }
  { MF_SA(sA[1][0], kf[2], 232); DMA_V1(NT - 2, 0, 2); SBAR(); }
  { MF_SV(sA[2][0], kf[2], qf2[2]); sB[2][2][2] = exp_s(ffma_s(sB[2][2][2], csc, nmc[2])); PIN(sB[2][2][2]); SBAR(); }
  { MF_SA(sA[0][0], kf[3], 204); kf[1] = KRD(0, 1, 1); SBAR(); }
  { MF_SA(sA[1][0], kf[3], 236); DMA_V1(NT - 2, 0, 3); SBAR(); }
  { MF_SV(sA[2][0], kf[3], qf2[3]); sB[2][2][3] = exp_s(ffma_s(sB[2][2][3], csc, nmc[2])); PIN(sB[2][2][3]); SBAR(); }
  { MF_SA(sA[0][0], kf[4], 208); kf[2] = KRD(0, 1, 2); SBAR(); }
  { MF_SA(sA[1][0], kf[4], 240); DMA_V1(NT - 2, 0, 4); SBAR(); }
  { MF_SV(sA[2][0], kf[4], qf2[4]); sB[2][3][0] = exp_s(ffma_s(sB[2][3][0], csc, nmc[2])); PIN(sB[2][3][0]); SBAR(); }
  { MF_SA(sA[0][0], kf[5], 212); kf[3] = KRD(0, 1, 3); SBAR(); }
  { MF_SA(sA[1][0], kf[5], 244); DMA_V1(NT - 2, 0, 5); SBAR(); }
  { MF_SV(sA[2][0], kf[5], qf2[5]); sB[2][3][1] = exp_s(ffma_s(sB[2][3][1], csc, nmc[2])); PIN(sB[2][3][1]); SBAR(); }
  { MF_SA(sA[0][0], kf[6], 216); kf[4] = KRD(0, 1, 4); SBAR(); }
  { MF_SA(sA[1][0], kf[6], 248); DMA_V1(NT - 2, 0, 6); SBAR(); }
  { MF_SV(sA[2][0], kf[6], qf2[6]); sB[2][3][2] = exp_s(ffma_s(sB[2][3][2], csc, nmc[2])); PIN(sB[2][3][2]); SBAR(); }
  { MF_SA(sA[0][0], kf[7], 220); kf[5] = KRD(0, 1, 5); SBAR(); }
  { MF_SA(sA[1][0], kf[7], 252); DMA_V1(NT - 2, 0, 7); SBAR(); }
  { MF_SV(sA[2][0], kf[7], qf2[7]); sB[2][3][3] = exp_s(ffma_s(sB[2][3][3], csc, nmc[2])); PIN(sB[2][3][3]); SBAR(); }
  { MF_S0A(sA[0][1], kf[0], 192); kf[6] = KRD(0, 1, 6); SBAR(); }
  { MF_S0A(sA[1][1], kf[0], 224); l[0] = fadd_s(l[0], sB[0][0][0]); PIN(l[0]); l[0] = fadd_s(l[0], sB[0][0][1]); PIN(l[0]); SBAR(); }
  { MF_S0V(sA[2][1], kf[0], qf2[0]); pw[0][0][0] = cvtpk(sB[0][0][0], sB[0][0][1]); PIN(pw[0][0][0]); SBAR(); }
  { MF_SA(sA[0][1], kf[1], 196); kf[7] = KRD(0, 1, 7); SBAR(); }
  { MF_SA(sA[1][1], kf[1], 228); l[0] = fadd_s(l[0], sB[0][0][2]); PIN(l[0]); l[0] = fadd_s(l[0], sB[0][0][3]); PIN(l[0]); SBAR(); }
  { MF_SV(sA[2][1], kf[1], qf2[1]); pw[0][0][1] = cvtpk(sB[0][0][2], sB[0][0][3]); PIN(pw[0][0][1]); SBAR(); }
  { MF_SA(sA[0][1], kf[2], 200); kf[0] = KRD(0, 2, 0); SBAR(); }
  { MF_SA(sA[1][1], kf[2], 232); l[0] = fadd_s(l[0], sB[0][1][0]); PIN(l[0]); l[0] = fadd_s(l[0], sB[0][1][1]); PIN(l[0]); SBAR(); }
  { MF_SV(sA[2][1], kf[2], qf2[2]); pw[0][0][2] = cvtpk(sB[0][1][0], sB[0][1][1]); PIN(pw[0][0][2]); SBAR(); }
  { MF_SA(sA[0][1], kf[3], 204); kf[1] = KRD(0, 2, 1); SBAR(); }
  { MF_SA(sA[1][1], kf[3], 236); l[0] = fadd_s(l[0], sB[0][1][2]); PIN(l[0]); l[0] = fadd_s(l[0], sB[0][1][3]); PIN(l[0]); SBAR(); }
  { MF_SV(sA[2][1], kf[3], qf2[3]); pw[0][0][3] = cvtpk(sB[0][1][2], sB[0][1][3]); PIN(pw[0][0][3]); SBAR(); }
  { MF_SA(sA[0][1], kf[4], 208); kf[2] = KRD(0, 2, 2); SBAR(); }
  { MF_SA(sA[1][1], kf[4], 240); l[0] = fadd_s(l[0], sB[0][2][0]); PIN(l[0]); l[0] = fadd_s(l[0], sB[0][2][1]); PIN(l[0]); SBAR(); }
  { MF_SV(sA[2][1], kf[4], qf2[4]); pw[0][1][0] = cvtpk(sB[0][2][0], sB[0][2][1]); PIN(pw[0][1][0]); SBAR(); }
  { MF_SA(sA[0][1], kf[5], 212); kf[3] = KRD(0, 2, 3); SBAR(); }
  { MF_SA(sA[1][1], kf[5], 244); l[0] = fadd_s(l[0], sB[0][2][2]); PIN(l[0]); l[0] = fadd_s(l[0], sB[0][2][3]); PIN(l[0]); SBAR(); }
  { MF_SV(sA[2][1], kf[5], qf2[5]); pw[0][1][1] = cvtpk(sB[0][2][2], sB[0][2][3]); PIN(pw[0][1][1]); SBAR(); }
  { MF_SA(sA[0][1], kf[6], 216); kf[4] = KRD(0, 2, 4); SBAR(); }
  { MF_SA(sA[1][1], kf[6], 248); l[0] = fadd_s(l[0], sB[0][3][0]); PIN(l[0]); l[0] = fadd_s(l[0], sB[0][3][1]); PIN(l[0]); SBAR(); }
  { MF_SV(sA[2][1], kf[6], qf2[6]); pw[0][1][2] = cvtpk(sB[0][3][0], sB[0][3][1]); PIN(pw[0][1][2]); SBAR(); }
  { MF_SA(sA[0][1], kf[7], 220); kf[5] = KRD(0, 2, 5); SBAR(); }
  { MF_SA(sA[1][1], kf[7], 252); l[0] = fadd_s(l[0], sB[0][3][2]); PIN(l[0]); l[0] = fadd_s(l[0], sB[0][3][3]); PIN(l[0]); SBAR(); }
  { MF_SV(sA[2][1], kf[7], qf2[7]); pw[0][1][3] = cvtpk(sB[0][3][2], sB[0][3][3]); PIN(pw[0][1][3]); SBAR(); }
  { MF_S0A(sA[0][2], kf[0], 192); kf[6] = KRD(0, 2, 6); SBAR(); }
  { MF_S0A(sA[1][2], kf[0], 224); l[1] = fadd_s(l[1], sB[1][0][0]); PIN(l[1]); l[1] = fadd_s(l[1], sB[1][0][1]); PIN(l[1]); SBAR(); }
  { MF_S0V(sA[2][2], kf[0], qf2[0]); pw[1][0][0] = cvtpk(sB[1][0][0], sB[1][0][1]); PIN(pw[1][0][0]); SBAR(); }
  { MF_SA(sA[0][2], kf[1], 196); kf[7] = KRD(0, 2, 7); SBAR(); }
  { MF_SA(sA[1][2], kf[1], 228); l[1] = fadd_s(l[1], sB[1][0][2]); PIN(l[1]); l[1] = fadd_s(l[1], sB[1][0][3]); PIN(l[1]); SBAR(); }
  { MF_SV(sA[2][2], kf[1], qf2[1]); pw[1][0][1] = cvtpk(sB[1][0][2], sB[1][0][3]); PIN(pw[1][0][1]); SBAR(); }
  { MF_SA(sA[0][2], kf[2], 200); kf[0] = KRD(0, 3, 0); SBAR(); }
  { MF_SA(sA[1][2], kf[2], 232); l[1] = fadd_s(l[1], sB[1][1][0]); PIN(l[1]); l[1] = fadd_s(l[1], sB[1][1][1]); PIN(l[1]); SBAR(); }
  { MF_SV(sA[2][2], kf[2], qf2[2]); pw[1][0][2] = cvtpk(sB[1][1][0], sB[1][1][1]); PIN(pw[1][0][2]); SBAR(); }
  { MF_SA(sA[0][2], kf[3], 204); kf[1] = KRD(0, 3, 1); SBAR(); }
  { MF_SA(sA[1][2], kf[3], 236); l[1] = fadd_s(l[1], sB[1][1][2]); PIN(l[1]); l[1] = fadd_s(l[1], sB[1][1][3]); PIN(l[1]); SBAR(); }
  { MF_SV(sA[2][2], kf[3], qf2[3]); pw[1][0][3] = cvtpk(sB[1][1][2], sB[1][1][3]); PIN(pw[1][0][3]); SBAR(); }
  { MF_SA(sA[0][2], kf[4], 208); kf[2] = KRD(0, 3, 2); SBAR(); }
  { MF_SA(sA[1][2], kf[4], 240); l[1] = fadd_s(l[1], sB[1][2][0]); PIN(l[1]); l[1] = fadd_s(l[1], sB[1][2][1]); PIN(l[1]); SBAR(); }
  { MF_SV(sA[2][2], kf[4], qf2[4]); pw[1][1][0] = cvtpk(sB[1][2][0], sB[1][2][1]); PIN(pw[1][1][0]); SBAR(); }
  { MF_SA(sA[0][2], kf[5], 212); kf[3] = KRD(0, 3, 3); SBAR(); }
  { MF_SA(sA[1][2], kf[5], 244); l[1] = fadd_s(l[1], sB[1][2][2]); PIN(l[1]); l[1] = fadd_s(l[1], sB[1][2][3]); PIN(l[1]); SBAR(); }
  { MF_SV(sA[2][2], kf[5], qf2[5]); pw[1][1][1] = cvtpk(sB[1][2][2], sB[1][2][3]); PIN(pw[1][1][1]); SBAR(); }
  { MF_SA(sA[0][2], kf[6], 216); kf[4] = KRD(0, 3, 4); SBAR(); }
  { MF_SA(sA[1][2], kf[6], 248); l[1] = fadd_s(l[1], sB[1][3][0]); PIN(l[1]); l[1] = fadd_s(l[1], sB[1][3][1]); PIN(l[1]); SBAR(); }
  { MF_SV(sA[2][2], kf[6], qf2[6]); pw[1][1][2] = cvtpk(sB[1][3][0], sB[1][3][1]); PIN(pw[1][1][2]); SBAR(); }
  { MF_SA(sA[0][2], kf[7], 220); kf[5] = KRD(0, 3, 5); SBAR(); }
  { MF_SA(sA[1][2], kf[7], 252); l[1] = fadd_s(l[1], sB[1][3][2]); PIN(l[1]); l[1] = fadd_s(l[1], sB[1][3][3]); PIN(l[1]); SBAR(); }
  { MF_SV(sA[2][2], kf[7], qf2[7]); pw[1][1][3] = cvtpk(sB[1][3][2], sB[1][3][3]); PIN(pw[1][1][3]); SBAR(); }
  { MF_S0A(sA[0][3], kf[0], 192); kf[6] = KRD(0, 3, 6); SBAR(); }
  { MF_S0A(sA[1][3], kf[0], 224); l[2] = fadd_s(l[2], sB[2][0][0]); PIN(l[2]); l[2] = fadd_s(l[2], sB[2][0][1]); PIN(l[2]); SBAR(); }
  { MF_S0V(sA[2][3], kf[0], qf2[0]); pw[2][0][0] = cvtpk(sB[2][0][0], sB[2][0][1]); PIN(pw[2][0][0]); SBAR(); }
  { MF_SA(sA[0][3], kf[1], 196); kf[7] = KRD(0, 3, 7); SBAR(); }
  { MF_SA(sA[1][3], kf[1], 228); l[2] = fadd_s(l[2], sB[2][0][2]); PIN(l[2]); l[2] = fadd_s(l[2], sB[2][0][3]); PIN(l[2]); SBAR(); }
  { MF_SV(sA[2][3], kf[1], qf2[1]); pw[2][0][1] = cvtpk(sB[2][0][2], sB[2][0][3]); PIN(pw[2][0][1]); SBAR(); }
  { MF_SA(sA[0][3], kf[2], 200); vf[0] = VRD(1, 0, 0); SBAR(); }
  { MF_SA(sA[1][3], kf[2], 232); l[2] = fadd_s(l[2], sB[2][1][0]); PIN(l[2]); l[2] = fadd_s(l[2], sB[2][1][1]); PIN(l[2]); SBAR(); }
  { MF_SV(sA[2][3], kf[2], qf2[2]); pw[2][0][2] = cvtpk(sB[2][1][0], sB[2][1][1]); PIN(pw[2][0][2]); SBAR(); }
  { MF_SA(sA[0][3], kf[3], 204); vf[1] = VRD(1, 0, 1); SBAR(); }
  { MF_SA(sA[1][3], kf[3], 236); l[2] = fadd_s(l[2], sB[2][1][2]); PIN(l[2]); l[2] = fadd_s(l[2], sB[2][1][3]); PIN(l[2]); SBAR(); }
  { MF_SV(sA[2][3], kf[3], qf2[3]); pw[2][0][3] = cvtpk(sB[2][1][2], sB[2][1][3]); PIN(pw[2][0][3]); SBAR(); }
  { MF_SA(sA[0][3], kf[4], 208); vf[2] = VRD(1, 0, 2); SBAR(); }
  { MF_SA(sA[1][3], kf[4], 240); l[2] = fadd_s(l[2], sB[2][2][0]); PIN(l[2]); l[2] = fadd_s(l[2], sB[2][2][1]); PIN(l[2]); SBAR(); }
  { MF_SV(sA[2][3], kf[4], qf2[4]); pw[2][1][0] = cvtpk(sB[2][2][0], sB[2][2][1]); PIN(pw[2][1][0]); SBAR(); }
  { MF_SA(sA[0][3], kf[5], 212); vf[3] = VRD(1, 0, 3); SBAR(); }
  { MF_SA(sA[1][3], kf[5], 244); l[2] = fadd_s(l[2], sB[2][2][2]); PIN(l[2]); l[2] = fadd_s(l[2], sB[2][2][3]); PIN(l[2]); SBAR(); }
  { MF_SV(sA[2][3], kf[5], qf2[5]); pw[2][1][1] = cvtpk(sB[2][2][2], sB[2][2][3]); PIN(pw[2][1][1]); SBAR(); }
  { MF_SA(sA[0][3], kf[6], 216); vf[4] = VRD(1, 0, 4); SBAR(); }
  { MF_SA(sA[1][3], kf[6], 248); l[2] = fadd_s(l[2], sB[2][3][0]); PIN(l[2]); l[2] = fadd_s(l[2], sB[2][3][1]); PIN(l[2]); SBAR(); }
  { MF_SV(sA[2][3], kf[6], qf2[6]); pw[2][1][2] = cvtpk(sB[2][3][0], sB[2][3][1]); PIN(pw[2][1][2]); SBAR(); }
  { MF_SA(sA[0][3], kf[7], 220); vf[5] = VRD(1, 0, 5); SBAR(); }
  { MF_SA(sA[1][3], kf[7], 252); l[2] = fadd_s(l[2], sB[2][3][2]); PIN(l[2]); l[2] = fadd_s(l[2], sB[2][3][3]); PIN(l[2]); SBAR(); }
  { MF_SV(sA[2][3], kf[7], qf2[7]); pw[2][1][3] = cvtpk(sB[2][3][2], sB[2][3][3]); PIN(pw[2][1][3]); SBAR(); }
  _Pragma("unroll") for (int sb = 0; sb < 3; ++sb) { pf[sb][0] = __builtin_bit_cast(bf16x8, pw[sb][0]); pf[sb][1] = __builtin_bit_cast(bf16x8, pw[sb][1]); PIN(pf[sb][0]); PIN(pf[sb][1]); }
  asm volatile("s_nop 1" ::: "memory"); SBAR();
  asm volatile("s_waitcnt vmcnt(16)\n\ts_barrier" ::: "memory");
  { MF_O(0, vf[0], pf[0][0]); vf[6] = VRD(1, 0, 6); SBAR(); }
  { MF_O(64, vf[0], pf[1][0]); mx[0] = max2f(sA[0][0][0], sA[0][0][1]); PIN(mx[0]); SBAR(); }
  { MF_O(128, vf[0], pf[2][0]); mx[1] = max2f(sA[1][0][0], sA[1][0][1]); PIN(mx[1]); SBAR(); }
  { MF_O(4, vf[1], pf[0][0]); vf[7] = VRD(1, 0, 7); SBAR(); }
  { MF_O(68, vf[1], pf[1][0]); mx[2] = max2f(sA[2][0][0], sA[2][0][1]); PIN(mx[2]); SBAR(); }
  { MF_O(132, vf[1], pf[2][0]); mx[0] = max3f(mx[0], sA[0][0][2], sA[0][0][3]); PIN(mx[0]); SBAR(); }
  { MF_O(8, vf[2], pf[0][0]); vf[0] = VRD(1, 0, 8); SBAR(); }
  { MF_O(72, vf[2], pf[1][0]); mx[1] = max3f(mx[1], sA[1][0][2], sA[1][0][3]); PIN(mx[1]); SBAR(); }
  { MF_O(136, vf[2], pf[2][0]); mx[2] = max3f(mx[2], sA[2][0][2], sA[2][0][3]); PIN(mx[2]); SBAR(); }
  { MF_O(12, vf[3], pf[0][0]); vf[1] = VRD(1, 0, 9); SBAR(); }
  { MF_O(76, vf[3], pf[1][0]); mx[0] = max3f(mx[0], sA[0][1][0], sA[0][1][1]); PIN(mx[0]); SBAR(); }
  { MF_O(140, vf[3], pf[2][0]); mx[1] = max3f(mx[1], sA[1][1][0], sA[1][1][1]); PIN(mx[1]); SBAR(); }
  { MF_O(16, vf[4], pf[0][0]); vf[2] = VRD(1, 0, 10); SBAR(); }
  { MF_O(80, vf[4], pf[1][0]); mx[2] = max3f(mx[2], sA[2][1][0], sA[2][1][1]); PIN(mx[2]); SBAR(); }
  { MF_O(144, vf[4], pf[2][0]); mx[0] = max3f(mx[0], sA[0][1][2], sA[0][1][3]); PIN(mx[0]); SBAR(); }
  { MF_O(20, vf[5], pf[0][0]); vf[3] = VRD(1, 0, 11); SBAR(); }
  { MF_O(84, vf[5], pf[1][0]); mx[1] = max3f(mx[1], sA[1][1][2], sA[1][1][3]); PIN(mx[1]); SBAR(); }
  { MF_O(148, vf[5], pf[2][0]); mx[2] = max3f(mx[2], sA[2][1][2], sA[2][1][3]); PIN(mx[2]); SBAR(); }
  { MF_O(24, vf[6], pf[0][0]); vf[4] = VRD(1, 0, 12); SBAR(); }
  { MF_O(88, vf[6], pf[1][0]); mx[0] = max3f(mx[0], sA[0][2][0], sA[0][2][1]); PIN(mx[0]); SBAR(); }
  { MF_O(152, vf[6], pf[2][0]); mx[1] = max3f(mx[1], sA[1][2][0], sA[1][2][1]); PIN(mx[1]); SBAR(); }
  { MF_O(28, vf[7], pf[0][0]); vf[5] = VRD(1, 0, 13); SBAR(); }
  { MF_O(92, vf[7], pf[1][0]); mx[2] = max3f(mx[2], sA[2][2][0], sA[2][2][1]); PIN(mx[2]); SBAR(); }
  { MF_O(156, vf[7], pf[2][0]); mx[0] = max3f(mx[0], sA[0][2][2], sA[0][2][3]); PIN(mx[0]); SBAR(); }
  { MF_O(32, vf[0], pf[0][0]); vf[6] = VRD(1, 0, 14); SBAR(); }
  { MF_O(96, vf[0], pf[1][0]); mx[1] = max3f(mx[1], sA[1][2][2], sA[1][2][3]); PIN(mx[1]); SBAR(); }
  { MF_O(160, vf[0], pf[2][0]); mx[2] = max3f(mx[2], sA[2][2][2], sA[2][2][3]); PIN(mx[2]); SBAR(); }
  { MF_O(36, vf[1], pf[0][0]); vf[7] = VRD(1, 0, 15); SBAR(); }
  { MF_O(100, vf[1], pf[1][0]); mx[0] = max3f(mx[0], sA[0][3][0], sA[0][3][1]); PIN(mx[0]); SBAR(); }
  { MF_O(164, vf[1], pf[2][0]); mx[1] = max3f(mx[1], sA[1][3][0], sA[1][3][1]); PIN(mx[1]); SBAR(); }
  { MF_O(40, vf[2], pf[0][0]); vf[0] = VRD(1, 1, 0); SBAR(); }
  { MF_O(104, vf[2], pf[1][0]); mx[2] = max3f(mx[2], sA[2][3][0], sA[2][3][1]); PIN(mx[2]); SBAR(); }
  { MF_O(168, vf[2], pf[2][0]); mx[0] = max3f(mx[0], sA[0][3][2], sA[0][3][3]); PIN(mx[0]); SBAR(); }
  { MF_O(44, vf[3], pf[0][0]); vf[1] = VRD(1, 1, 1); SBAR(); }
  { MF_O(108, vf[3], pf[1][0]); mx[1] = max3f(mx[1], sA[1][3][2], sA[1][3][3]); PIN(mx[1]); SBAR(); }
  { MF_O(172, vf[3], pf[2][0]); mx[2] = max3f(mx[2], sA[2][3][2], sA[2][3][3]); PIN(mx[2]); SBAR(); }
  { MF_O(48, vf[4], pf[0][0]); vf[2] = VRD(1, 1, 2); SBAR(); }
  { MF_O(112, vf[4], pf[1][0]); mx[0] = xmax16(mx[0]); PIN(mx[0]); SBAR(); }
  { MF_O(176, vf[4], pf[2][0]); mx[1] = xmax16(mx[1]); PIN(mx[1]); SBAR(); }
  { MF_O(52, vf[5], pf[0][0]); vf[3] = VRD(1, 1, 3); SBAR(); }
  { MF_O(116, vf[5], pf[1][0]); mx[2] = xmax16(mx[2]); PIN(mx[2]); SBAR(); }
  { MF_O(180, vf[5], pf[2][0]); mx[0] = xmax32(mx[0]); PIN(mx[0]); SBAR(); }
  { MF_O(56, vf[6], pf[0][0]); vf[4] = VRD(1, 1, 4); SBAR(); }
  { MF_O(120, vf[6], pf[1][0]); mx[1] = xmax32(mx[1]); PIN(mx[1]); SBAR(); }
  { MF_O(184, vf[6], pf[2][0]); mx[2] = xmax32(mx[2]); PIN(mx[2]); SBAR(); }
  { MF_O(60, vf[7], pf[0][0]); vf[5] = VRD(1, 1, 5); SBAR(); }
  {
    const bool grow = (mx[0] - m[0] > THR) | (mx[1] - m[1] > THR) | (mx[2] - m[2] > THR);
    if (__builtin_expect(__any((int)grow), 0)) {
#pragma unroll
      for (int sb = 0; sb < 3; ++sb) { const float mn = fmaxf(m[sb], mx[sb]);
        const float al = __builtin_amdgcn_exp2f((m[sb] - mn) * CSC); m[sb] = mn; nmc[sb] = -mn * CSC; l[sb] *= al; alv[sb] = al; }
      pend = 1;
    }
    SBAR();
  }
  { MF_O(124, vf[7], pf[1][0]); sA[0][0][0] = exp_s(ffma_s(sA[0][0][0], csc, nmc[0])); PIN(sA[0][0][0]); SBAR(); }
  { MF_O(188, vf[7], pf[2][0]); sA[0][0][1] = exp_s(ffma_s(sA[0][0][1], csc, nmc[0])); PIN(sA[0][0][1]); SBAR(); }
  { MF_O(0, vf[0], pf[0][1]); vf[6] = VRD(1, 1, 6); sA[0][0][2] = exp_s(ffma_s(sA[0][0][2], csc, nmc[0])); PIN(sA[0][0][2]); SBAR(); }
  { MF_O(64, vf[0], pf[1][1]); sA[0][0][3] = exp_s(ffma_s(sA[0][0][3], csc, nmc[0])); PIN(sA[0][0][3]); SBAR(); }
  { MF_O(128, vf[0], pf[2][1]); sA[0][1][0] = exp_s(ffma_s(sA[0][1][0], csc, nmc[0])); PIN(sA[0][1][0]); SBAR(); }
  { MF_O(4, vf[1], pf[0][1]); vf[7] = VRD(1, 1, 7); sA[0][1][1] = exp_s(ffma_s(sA[0][1][1], csc, nmc[0])); PIN(sA[0][1][1]); SBAR(); }
  { MF_O(68, vf[1], pf[1][1]); sA[0][1][2] = exp_s(ffma_s(sA[0][1][2], csc, nmc[0])); PIN(sA[0][1][2]); SBAR(); }
  { MF_O(132, vf[1], pf[2][1]); sA[0][1][3] = exp_s(ffma_s(sA[0][1][3], csc, nmc[0])); PIN(sA[0][1][3]); SBAR(); }
  { MF_O(8, vf[2], pf[0][1]); vf[0] = VRD(1, 1, 8); sA[0][2][0] = exp_s(ffma_s(sA[0][2][0], csc, nmc[0])); PIN(sA[0][2][0]); SBAR(); }
  { MF_O(72, vf[2], pf[1][1]); sA[0][2][1] = exp_s(ffma_s(sA[0][2][1], csc, nmc[0])); PIN(sA[0][2][1]); SBAR(); }
  { MF_O(136, vf[2], pf[2][1]); sA[0][2][2] = exp_s(ffma_s(sA[0][2][2], csc, nmc[0])); PIN(sA[0][2][2]); SBAR(); }
  { MF_O(12, vf[3], pf[0][1]); vf[1] = VRD(1, 1, 9); sA[0][2][3] = exp_s(ffma_s(sA[0][2][3], csc, nmc[0])); PIN(sA[0][2][3]); SBAR(); }
  { MF_O(76, vf[3], pf[1][1]); sA[0][3][0] = exp_s(ffma_s(sA[0][3][0], csc, nmc[0])); PIN(sA[0][3][0]); SBAR(); }
  { MF_O(140, vf[3], pf[2][1]); sA[0][3][1] = exp_s(ffma_s(sA[0][3][1], csc, nmc[0])); PIN(sA[0][3][1]); SBAR(); }
  { MF_O(16, vf[4], pf[0][1]); vf[2] = VRD(1, 1, 10); sA[0][3][2] = exp_s(ffma_s(sA[0][3][2], csc, nmc[0])); PIN(sA[0][3][2]); SBAR(); }
  { MF_O(80, vf[4], pf[1][1]); sA[0][3][3] = exp_s(ffma_s(sA[0][3][3], csc, nmc[0])); PIN(sA[0][3][3]); SBAR(); }
  { MF_O(144, vf[4], pf[2][1]); sA[1][0][0] = exp_s(ffma_s(sA[1][0][0], csc, nmc[1])); PIN(sA[1][0][0]); SBAR(); }
  { MF_O(20, vf[5], pf[0][1]); vf[3] = VRD(1, 1, 11); sA[1][0][1] = exp_s(ffma_s(sA[1][0][1], csc, nmc[1])); PIN(sA[1][0][1]); SBAR(); }
  { MF_O(84, vf[5], pf[1][1]); sA[1][0][2] = exp_s(ffma_s(sA[1][0][2], csc, nmc[1])); PIN(sA[1][0][2]); SBAR(); }
  { MF_O(148, vf[5], pf[2][1]); sA[1][0][3] = exp_s(ffma_s(sA[1][0][3], csc, nmc[1])); PIN(sA[1][0][3]); SBAR(); }
  { MF_O(24, vf[6], pf[0][1]); vf[4] = VRD(1, 1, 12); SBAR(); }
  { MF_O(88, vf[6], pf[1][1]); sA[1][1][0] = exp_s(ffma_s(sA[1][1][0], csc, nmc[1])); PIN(sA[1][1][0]); SBAR(); }
  { MF_O(152, vf[6], pf[2][1]); sA[1][1][1] = exp_s(ffma_s(sA[1][1][1], csc, nmc[1])); PIN(sA[1][1][1]); SBAR(); }
  { MF_O(28, vf[7], pf[0][1]); vf[5] = VRD(1, 1, 13); SBAR(); }
  { MF_O(92, vf[7], pf[1][1]); sA[1][1][2] = exp_s(ffma_s(sA[1][1][2], csc, nmc[1])); PIN(sA[1][1][2]); SBAR(); }
  { MF_O(156, vf[7], pf[2][1]); sA[1][1][3] = exp_s(ffma_s(sA[1][1][3], csc, nmc[1])); PIN(sA[1][1][3]); SBAR(); }
  { MF_O(32, vf[0], pf[0][1]); vf[6] = VRD(1, 1, 14); SBAR(); }
  { MF_O(96, vf[0], pf[1][1]); sA[1][2][0] = exp_s(ffma_s(sA[1][2][0], csc, nmc[1])); PIN(sA[1][2][0]); SBAR(); }
  { MF_O(160, vf[0], pf[2][1]); sA[1][2][1] = exp_s(ffma_s(sA[1][2][1], csc, nmc[1])); PIN(sA[1][2][1]); SBAR(); }
  { MF_O(36, vf[1], pf[0][1]); vf[7] = VRD(1, 1, 15); SBAR(); }
  { MF_O(100, vf[1], pf[1][1]); sA[1][2][2] = exp_s(ffma_s(sA[1][2][2], csc, nmc[1])); PIN(sA[1][2][2]); SBAR(); }
  { MF_O(164, vf[1], pf[2][1]); sA[1][2][3] = exp_s(ffma_s(sA[1][2][3], csc, nmc[1])); PIN(sA[1][2][3]); SBAR(); }
  { MF_O(40, vf[2], pf[0][1]);  SBAR(); }
  { MF_O(104, vf[2], pf[1][1]); sA[1][3][0] = exp_s(ffma_s(sA[1][3][0], csc, nmc[1])); PIN(sA[1][3][0]); SBAR(); }
  { MF_O(168, vf[2], pf[2][1]); sA[1][3][1] = exp_s(ffma_s(sA[1][3][1], csc, nmc[1])); PIN(sA[1][3][1]); SBAR(); }
  { MF_O(44, vf[3], pf[0][1]);  SBAR(); }
  { MF_O(108, vf[3], pf[1][1]); sA[1][3][2] = exp_s(ffma_s(sA[1][3][2], csc, nmc[1])); PIN(sA[1][3][2]); SBAR(); }
  { MF_O(172, vf[3], pf[2][1]); sA[1][3][3] = exp_s(ffma_s(sA[1][3][3], csc, nmc[1])); PIN(sA[1][3][3]); SBAR(); }
  { MF_O(48, vf[4], pf[0][1]);  SBAR(); }
  { MF_O(112, vf[4], pf[1][1]); sA[2][0][0] = exp_s(ffma_s(sA[2][0][0], csc, nmc[2])); PIN(sA[2][0][0]); SBAR(); }
  { MF_O(176, vf[4], pf[2][1]); sA[2][0][1] = exp_s(ffma_s(sA[2][0][1], csc, nmc[2])); PIN(sA[2][0][1]); SBAR(); }
  { MF_O(52, vf[5], pf[0][1]);  SBAR(); }
  { MF_O(116, vf[5], pf[1][1]); sA[2][0][2] = exp_s(ffma_s(sA[2][0][2], csc, nmc[2])); PIN(sA[2][0][2]); SBAR(); }
  { MF_O(180, vf[5], pf[2][1]); sA[2][0][3] = exp_s(ffma_s(sA[2][0][3], csc, nmc[2])); PIN(sA[2][0][3]); SBAR(); }
  { MF_O(56, vf[6], pf[0][1]);  SBAR(); }
  { MF_O(120, vf[6], pf[1][1]); sA[2][1][0] = exp_s(ffma_s(sA[2][1][0], csc, nmc[2])); PIN(sA[2][1][0]); SBAR(); }
  { MF_O(184, vf[6], pf[2][1]); sA[2][1][1] = exp_s(ffma_s(sA[2][1][1], csc, nmc[2])); PIN(sA[2][1][1]); SBAR(); }
  { MF_O(60, vf[7], pf[0][1]);  SBAR(); }
  { MF_O(124, vf[7], pf[1][1]); sA[2][1][2] = exp_s(ffma_s(sA[2][1][2], csc, nmc[2])); PIN(sA[2][1][2]); SBAR(); }
  { MF_O(188, vf[7], pf[2][1]); sA[2][1][3] = exp_s(ffma_s(sA[2][1][3], csc, nmc[2])); PIN(sA[2][1][3]); SBAR(); }
  if (__builtin_expect(pend, 0)) {
    asm volatile("s_nop 7\n\ts_nop 7" ::: "memory");
      { float t0, t1; asm volatile("v_accvgpr_read_b32 %0, a0\n\tv_mul_f32 %0, %2, %0\n\tv_accvgpr_write_b32 a0, %0\n\t" "v_accvgpr_read_b32 %1, a1\n\tv_mul_f32 %1, %2, %1\n\tv_accvgpr_write_b32 a1, %1\n\t" "v_accvgpr_read_b32 %0, a2\n\tv_mul_f32 %0, %2, %0\n\tv_accvgpr_write_b32 a2, %0\n\t" "v_accvgpr_read_b32 %1, a3\n\tv_mul_f32 %1, %2, %1\n\tv_accvgpr_write_b32 a3, %1\n\t" "v_accvgpr_read_b32 %0, a4\n\tv_mul_f32 %0, %2, %0\n\tv_accvgpr_write_b32 a4, %0\n\t" "v_accvgpr_read_b32 %1, a5\n\tv_mul_f32 %1, %2, %1\n\tv_accvgpr_write_b32 a5, %1\n\t" "v_accvgpr_read_b32 %0, a6\n\tv_mul_f32 %0, %2, %0\n\tv_accvgpr_write_b32 a6, %0\n\t" "v_accvgpr_read_b32 %1, a7\n\tv_mul_f32 %1, %2, %1\n\tv_accvgpr_write_b32 a7, %1\n\t" "v_accvgpr_read_b32 %0, a8\n\tv_mul_f32 %0, %2, %0\n\tv_accvgpr_write_b32 a8, %0\n\t" "v_accvgpr_read_b32 %1, a9\n\tv_mul_f32 %1, %2, %1\n\tv_accvgpr_write_b32 a9, %1\n\t" "v_accvgpr_read_b32 %0, a10\n\tv_mul_f32 %0, %2, %0\n\tv_accvgpr_write_b32 a10, %0\n\t" "v_accvgpr_read_b32 %1, a11\n\tv_mul_f32 %1, %2, %1\n\tv_accvgpr_write_b32 a11, %1\n\t" "v_accvgpr_read_b32 %0, a12\n\tv_mul_f32 %0, %2, %0\n\tv_accvgpr_write_b32 a12, %0\n\t" "v_accvgpr_read_b32 %1, a13\n\tv_mul_f32 %1, %2, %1\n\tv_accvgpr_write_b32 a13, %1\n\t" "v_accvgpr_read_b32 %0, a14\n\tv_mul_f32 %0, %2, %0\n\tv_accvgpr_write_b32 a14, %0\n\t" "v_accvgpr_read_b32 %1, a15\n\tv_mul_f32 %1, %2, %1\n\tv_accvgpr_write_b32 a15, %1\n\t" "v_accvgpr_read_b32 %0, a16\n\tv_mul_f32 %0, %2, %0\n\tv_accvgpr_write_b32 a16, %0\n\t" "v_accvgpr_read_b32 %1, a17\n\tv_mul_f32 %1, %2, %1\n\tv_accvgpr_write_b32 a17, %1\n\t" "v_accvgpr_read_b32 %0, a18\n\tv_mul_f32 %0, %2, %0\n\tv_accvgpr_write_b32 a18, %0\n\t" "v_accvgpr_read_b32 %1, a19\n\tv_mul_f32 %1, %2, %1\n\tv_accvgpr_write_b32 a19, %1\n\t" "v_accvgpr_read_b32 %0, a20\n\tv_mul_f32 %0, %2, %0\n\tv_accvgpr_write_b32 a20, %0\n\t" "v_accvgpr_read_b32 %1, a21\n\tv_mul_f32 %1, %2, %1\n\tv_accvgpr_write_b32 a21, %1\n\t" "v_accvgpr_read_b32 %0, a22\n\tv_mul_f32 %0, %2, %0\n\tv_accvgpr_write_b32 a22, %0\n\t" "v_accvgpr_read_b32 %1, a23\n\tv_mul_f32 %1, %2, %1\n\tv_accvgpr_write_b32 a23, %1\n\t" "v_accvgpr_read_b32 %0, a24\n\tv_mul_f32 %0, %2, %0\n\tv_accvgpr_write_b32 a24, %0\n\t" "v_accvgpr_read_b32 %1, a25\n\tv_mul_f32 %1, %2, %1\n\tv_accvgpr_write_b32 a25, %1\n\t" "v_accvgpr_read_b32 %0, a26\n\tv_mul_f32 %0, %2, %0\n\tv_accvgpr_write_b32 a26, %0\n\t" "v_accvgpr_read_b32 %1, a27\n\tv_mul_f32 %1, %2, %1\n\tv_accvgpr_write_b32 a27, %1\n\t" "v_accvgpr_read_b32 %0, a28\n\tv_mul_f32 %0, %2, %0\n\tv_accvgpr_write_b32 a28, %0\n\t" "v_accvgpr_read_b32 %1, a29\n\tv_mul_f32 %1, %2, %1\n\tv_accvgpr_write_b32 a29, %1\n\t" "v_accvgpr_read_b32 %0, a30\n\tv_mul_f32 %0, %2, %0\n\tv_accvgpr_write_b32 a30, %0\n\t" "v_accvgpr_read_b32 %1, a31\n\tv_mul_f32 %1, %2, %1\n\tv_accvgpr_write_b32 a31, %1\n\t" "v_accvgpr_read_b32 %0, a32\n\tv_mul_f32 %0, %2, %0\n\tv_accvgpr_write_b32 a32, %0\n\t" "v_accvgpr_read_b32 %1, a33\n\tv_mul_f32 %1, %2, %1\n\tv_accvgpr_write_b32 a33, %1\n\t" "v_accvgpr_read_b32 %0, a34\n\tv_mul_f32 %0, %2, %0\n\tv_accvgpr_write_b32 a34, %0\n\t" "v_accvgpr_read_b32 %1, a35\n\tv_mul_f32 %1, %2, %1\n\tv_accvgpr_write_b32 a35, %1\n\t" "v_accvgpr_read_b32 %0, a36\n\tv_mul_f32 %0, %2, %0\n\tv_accvgpr_write_b32 a36, %0\n\t" "v_accvgpr_read_b32 %1, a37\n\tv_mul_f32 %1, %2, %1\n\tv_accvgpr_write_b32 a37, %1\n\t" "v_accvgpr_read_b32 %0, a38\n\tv_mul_f32 %0, %2, %0\n\tv_accvgpr_write_b32 a38, %0\n\t" "v_accvgpr_read_b32 %1, a39\n\tv_mul_f32 %1, %2, %1\n\tv_accvgpr_write_b32 a39, %1\n\t" "v_accvgpr_read_b32 %0, a40\n\tv_mul_f32 %0, %2, %0\n\tv_accvgpr_write_b32 a40, %0\n\t" "v_accvgpr_read_b32 %1, a41\n\tv_mul_f32 %1, %2, %1\n\tv_accvgpr_write_b32 a41, %1\n\t" "v_accvgpr_read_b32 %0, a42\n\tv_mul_f32 %0, %2, %0\n\tv_accvgpr_write_b32 a42, %0\n\t" "v_accvgpr_read_b32 %1, a43\n\tv_mul_f32 %1, %2, %1\n\tv_accvgpr_write_b32 a43, %1\n\t" "v_accvgpr_read_b32 %0, a44\n\tv_mul_f32 %0, %2, %0\n\tv_accvgpr_write_b32 a44, %0\n\t" "v_accvgpr_read_b32 %1, a45\n\tv_mul_f32 %1, %2, %1\n\tv_accvgpr_write_b32 a45, %1\n\t" "v_accvgpr_read_b32 %0, a46\n\tv_mul_f32 %0, %2, %0\n\tv_accvgpr_write_b32 a46, %0\n\t" "v_accvgpr_read_b32 %1, a47\n\tv_mul_f32 %1, %2, %1\n\tv_accvgpr_write_b32 a47, %1\n\t" "v_accvgpr_read_b32 %0, a48\n\tv_mul_f32 %0, %2, %0\n\tv_accvgpr_write_b32 a48, %0\n\t" "v_accvgpr_read_b32 %1, a49\n\tv_mul_f32 %1, %2, %1\n\tv_accvgpr_write_b32 a49, %1\n\t" "v_accvgpr_read_b32 %0, a50\n\tv_mul_f32 %0, %2, %0\n\tv_accvgpr_write_b32 a50, %0\n\t" "v_accvgpr_read_b32 %1, a51\n\tv_mul_f32 %1, %2, %1\n\tv_accvgpr_write_b32 a51, %1\n\t" "v_accvgpr_read_b32 %0, a52\n\tv_mul_f32 %0, %2, %0\n\tv_accvgpr_write_b32 a52, %0\n\t" "v_accvgpr_read_b32 %1, a53\n\tv_mul_f32 %1, %2, %1\n\tv_accvgpr_write_b32 a53, %1\n\t" "v_accvgpr_read_b32 %0, a54\n\tv_mul_f32 %0, %2, %0\n\tv_accvgpr_write_b32 a54, %0\n\t" "v_accvgpr_read_b32 %1, a55\n\tv_mul_f32 %1, %2, %1\n\tv_accvgpr_write_b32 a55, %1\n\t" "v_accvgpr_read_b32 %0, a56\n\tv_mul_f32 %0, %2, %0\n\tv_accvgpr_write_b32 a56, %0\n\t" "v_accvgpr_read_b32 %1, a57\n\tv_mul_f32 %1, %2, %1\n\tv_accvgpr_write_b32 a57, %1\n\t" "v_accvgpr_read_b32 %0, a58\n\tv_mul_f32 %0, %2, %0\n\tv_accvgpr_write_b32 a58, %0\n\t" "v_accvgpr_read_b32 %1, a59\n\tv_mul_f32 %1, %2, %1\n\tv_accvgpr_write_b32 a59, %1\n\t" "v_accvgpr_read_b32 %0, a60\n\tv_mul_f32 %0, %2, %0\n\tv_accvgpr_write_b32 a60, %0\n\t" "v_accvgpr_read_b32 %1, a61\n\tv_mul_f32 %1, %2, %1\n\tv_accvgpr_write_b32 a61, %1\n\t" "v_accvgpr_read_b32 %0, a62\n\tv_mul_f32 %0, %2, %0\n\tv_accvgpr_write_b32 a62, %0\n\t" "v_accvgpr_read_b32 %1, a63\n\tv_mul_f32 %1, %2, %1\n\tv_accvgpr_write_b32 a63, %1\n\t" : "=&v"(t0), "=&v"(t1) : "v"(alv[0]) : ACLOB); }
      { float t0, t1; asm volatile("v_accvgpr_read_b32 %0, a64\n\tv_mul_f32 %0, %2, %0\n\tv_accvgpr_write_b32 a64, %0\n\t" "v_accvgpr_read_b32 %1, a65\n\tv_mul_f32 %1, %2, %1\n\tv_accvgpr_write_b32 a65, %1\n\t" "v_accvgpr_read_b32 %0, a66\n\tv_mul_f32 %0, %2, %0\n\tv_accvgpr_write_b32 a66, %0\n\t" "v_accvgpr_read_b32 %1, a67\n\tv_mul_f32 %1, %2, %1\n\tv_accvgpr_write_b32 a67, %1\n\t" "v_accvgpr_read_b32 %0, a68\n\tv_mul_f32 %0, %2, %0\n\tv_accvgpr_write_b32 a68, %0\n\t" "v_accvgpr_read_b32 %1, a69\n\tv_mul_f32 %1, %2, %1\n\tv_accvgpr_write_b32 a69, %1\n\t" "v_accvgpr_read_b32 %0, a70\n\tv_mul_f32 %0, %2, %0\n\tv_accvgpr_write_b32 a70, %0\n\t" "v_accvgpr_read_b32 %1, a71\n\tv_mul_f32 %1, %2, %1\n\tv_accvgpr_write_b32 a71, %1\n\t" "v_accvgpr_read_b32 %0, a72\n\tv_mul_f32 %0, %2, %0\n\tv_accvgpr_write_b32 a72, %0\n\t" "v_accvgpr_read_b32 %1, a73\n\tv_mul_f32 %1, %2, %1\n\tv_accvgpr_write_b32 a73, %1\n\t" "v_accvgpr_read_b32 %0, a74\n\tv_mul_f32 %0, %2, %0\n\tv_accvgpr_write_b32 a74, %0\n\t" "v_accvgpr_read_b32 %1, a75\n\tv_mul_f32 %1, %2, %1\n\tv_accvgpr_write_b32 a75, %1\n\t" "v_accvgpr_read_b32 %0, a76\n\tv_mul_f32 %0, %2, %0\n\tv_accvgpr_write_b32 a76, %0\n\t" "v_accvgpr_read_b32 %1, a77\n\tv_mul_f32 %1, %2, %1\n\tv_accvgpr_write_b32 a77, %1\n\t" "v_accvgpr_read_b32 %0, a78\n\tv_mul_f32 %0, %2, %0\n\tv_accvgpr_write_b32 a78, %0\n\t" "v_accvgpr_read_b32 %1, a79\n\tv_mul_f32 %1, %2, %1\n\tv_accvgpr_write_b32 a79, %1\n\t" "v_accvgpr_read_b32 %0, a80\n\tv_mul_f32 %0, %2, %0\n\tv_accvgpr_write_b32 a80, %0\n\t" "v_accvgpr_read_b32 %1, a81\n\tv_mul_f32 %1, %2, %1\n\tv_accvgpr_write_b32 a81, %1\n\t" "v_accvgpr_read_b32 %0, a82\n\tv_mul_f32 %0, %2, %0\n\tv_accvgpr_write_b32 a82, %0\n\t" "v_accvgpr_read_b32 %1, a83\n\tv_mul_f32 %1, %2, %1\n\tv_accvgpr_write_b32 a83, %1\n\t" "v_accvgpr_read_b32 %0, a84\n\tv_mul_f32 %0, %2, %0\n\tv_accvgpr_write_b32 a84, %0\n\t" "v_accvgpr_read_b32 %1, a85\n\tv_mul_f32 %1, %2, %1\n\tv_accvgpr_write_b32 a85, %1\n\t" "v_accvgpr_read_b32 %0, a86\n\tv_mul_f32 %0, %2, %0\n\tv_accvgpr_write_b32 a86, %0\n\t" "v_accvgpr_read_b32 %1, a87\n\tv_mul_f32 %1, %2, %1\n\tv_accvgpr_write_b32 a87, %1\n\t" "v_accvgpr_read_b32 %0, a88\n\tv_mul_f32 %0, %2, %0\n\tv_accvgpr_write_b32 a88, %0\n\t" "v_accvgpr_read_b32 %1, a89\n\tv_mul_f32 %1, %2, %1\n\tv_accvgpr_write_b32 a89, %1\n\t" "v_accvgpr_read_b32 %0, a90\n\tv_mul_f32 %0, %2, %0\n\tv_accvgpr_write_b32 a90, %0\n\t" "v_accvgpr_read_b32 %1, a91\n\tv_mul_f32 %1, %2, %1\n\tv_accvgpr_write_b32 a91, %1\n\t" "v_accvgpr_read_b32 %0, a92\n\tv_mul_f32 %0, %2, %0\n\tv_accvgpr_write_b32 a92, %0\n\t" "v_accvgpr_read_b32 %1, a93\n\tv_mul_f32 %1, %2, %1\n\tv_accvgpr_write_b32 a93, %1\n\t" "v_accvgpr_read_b32 %0, a94\n\tv_mul_f32 %0, %2, %0\n\tv_accvgpr_write_b32 a94, %0\n\t" "v_accvgpr_read_b32 %1, a95\n\tv_mul_f32 %1, %2, %1\n\tv_accvgpr_write_b32 a95, %1\n\t" "v_accvgpr_read_b32 %0, a96\n\tv_mul_f32 %0, %2, %0\n\tv_accvgpr_write_b32 a96, %0\n\t" "v_accvgpr_read_b32 %1, a97\n\tv_mul_f32 %1, %2, %1\n\tv_accvgpr_write_b32 a97, %1\n\t" "v_accvgpr_read_b32 %0, a98\n\tv_mul_f32 %0, %2, %0\n\tv_accvgpr_write_b32 a98, %0\n\t" "v_accvgpr_read_b32 %1, a99\n\tv_mul_f32 %1, %2, %1\n\tv_accvgpr_write_b32 a99, %1\n\t" "v_accvgpr_read_b32 %0, a100\n\tv_mul_f32 %0, %2, %0\n\tv_accvgpr_write_b32 a100, %0\n\t" "v_accvgpr_read_b32 %1, a101\n\tv_mul_f32 %1, %2, %1\n\tv_accvgpr_write_b32 a101, %1\n\t" "v_accvgpr_read_b32 %0, a102\n\tv_mul_f32 %0, %2, %0\n\tv_accvgpr_write_b32 a102, %0\n\t" "v_accvgpr_read_b32 %1, a103\n\tv_mul_f32 %1, %2, %1\n\tv_accvgpr_write_b32 a103, %1\n\t" "v_accvgpr_read_b32 %0, a104\n\tv_mul_f32 %0, %2, %0\n\tv_accvgpr_write_b32 a104, %0\n\t" "v_accvgpr_read_b32 %1, a105\n\tv_mul_f32 %1, %2, %1\n\tv_accvgpr_write_b32 a105, %1\n\t" "v_accvgpr_read_b32 %0, a106\n\tv_mul_f32 %0, %2, %0\n\tv_accvgpr_write_b32 a106, %0\n\t" "v_accvgpr_read_b32 %1, a107\n\tv_mul_f32 %1, %2, %1\n\tv_accvgpr_write_b32 a107, %1\n\t" "v_accvgpr_read_b32 %0, a108\n\tv_mul_f32 %0, %2, %0\n\tv_accvgpr_write_b32 a108, %0\n\t" "v_accvgpr_read_b32 %1, a109\n\tv_mul_f32 %1, %2, %1\n\tv_accvgpr_write_b32 a109, %1\n\t" "v_accvgpr_read_b32 %0, a110\n\tv_mul_f32 %0, %2, %0\n\tv_accvgpr_write_b32 a110, %0\n\t" "v_accvgpr_read_b32 %1, a111\n\tv_mul_f32 %1, %2, %1\n\tv_accvgpr_write_b32 a111, %1\n\t" "v_accvgpr_read_b32 %0, a112\n\tv_mul_f32 %0, %2, %0\n\tv_accvgpr_write_b32 a112, %0\n\t" "v_accvgpr_read_b32 %1, a113\n\tv_mul_f32 %1, %2, %1\n\tv_accvgpr_write_b32 a113, %1\n\t" "v_accvgpr_read_b32 %0, a114\n\tv_mul_f32 %0, %2, %0\n\tv_accvgpr_write_b32 a114, %0\n\t" "v_accvgpr_read_b32 %1, a115\n\tv_mul_f32 %1, %2, %1\n\tv_accvgpr_write_b32 a115, %1\n\t" "v_accvgpr_read_b32 %0, a116\n\tv_mul_f32 %0, %2, %0\n\tv_accvgpr_write_b32 a116, %0\n\t" "v_accvgpr_read_b32 %1, a117\n\tv_mul_f32 %1, %2, %1\n\tv_accvgpr_write_b32 a117, %1\n\t" "v_accvgpr_read_b32 %0, a118\n\tv_mul_f32 %0, %2, %0\n\tv_accvgpr_write_b32 a118, %0\n\t" "v_accvgpr_read_b32 %1, a119\n\tv_mul_f32 %1, %2, %1\n\tv_accvgpr_write_b32 a119, %1\n\t" "v_accvgpr_read_b32 %0, a120\n\tv_mul_f32 %0, %2, %0\n\tv_accvgpr_write_b32 a120, %0\n\t" "v_accvgpr_read_b32 %1, a121\n\tv_mul_f32 %1, %2, %1\n\tv_accvgpr_write_b32 a121, %1\n\t" "v_accvgpr_read_b32 %0, a122\n\tv_mul_f32 %0, %2, %0\n\tv_accvgpr_write_b32 a122, %0\n\t" "v_accvgpr_read_b32 %1, a123\n\tv_mul_f32 %1, %2, %1\n\tv_accvgpr_write_b32 a123, %1\n\t" "v_accvgpr_read_b32 %0, a124\n\tv_mul_f32 %0, %2, %0\n\tv_accvgpr_write_b32 a124, %0\n\t" "v_accvgpr_read_b32 %1, a125\n\tv_mul_f32 %1, %2, %1\n\tv_accvgpr_write_b32 a125, %1\n\t" "v_accvgpr_read_b32 %0, a126\n\tv_mul_f32 %0, %2, %0\n\tv_accvgpr_write_b32 a126, %0\n\t" "v_accvgpr_read_b32 %1, a127\n\tv_mul_f32 %1, %2, %1\n\tv_accvgpr_write_b32 a127, %1\n\t" : "=&v"(t0), "=&v"(t1) : "v"(alv[1]) : ACLOB); }
      { float t0, t1; asm volatile("v_accvgpr_read_b32 %0, a128\n\tv_mul_f32 %0, %2, %0\n\tv_accvgpr_write_b32 a128, %0\n\t" "v_accvgpr_read_b32 %1, a129\n\tv_mul_f32 %1, %2, %1\n\tv_accvgpr_write_b32 a129, %1\n\t" "v_accvgpr_read_b32 %0, a130\n\tv_mul_f32 %0, %2, %0\n\tv_accvgpr_write_b32 a130, %0\n\t" "v_accvgpr_read_b32 %1, a131\n\tv_mul_f32 %1, %2, %1\n\tv_accvgpr_write_b32 a131, %1\n\t" "v_accvgpr_read_b32 %0, a132\n\tv_mul_f32 %0, %2, %0\n\tv_accvgpr_write_b32 a132, %0\n\t" "v_accvgpr_read_b32 %1, a133\n\tv_mul_f32 %1, %2, %1\n\tv_accvgpr_write_b32 a133, %1\n\t" "v_accvgpr_read_b32 %0, a134\n\tv_mul_f32 %0, %2, %0\n\tv_accvgpr_write_b32 a134, %0\n\t" "v_accvgpr_read_b32 %1, a135\n\tv_mul_f32 %1, %2, %1\n\tv_accvgpr_write_b32 a135, %1\n\t" "v_accvgpr_read_b32 %0, a136\n\tv_mul_f32 %0, %2, %0\n\tv_accvgpr_write_b32 a136, %0\n\t" "v_accvgpr_read_b32 %1, a137\n\tv_mul_f32 %1, %2, %1\n\tv_accvgpr_write_b32 a137, %1\n\t" "v_accvgpr_read_b32 %0, a138\n\tv_mul_f32 %0, %2, %0\n\tv_accvgpr_write_b32 a138, %0\n\t" "v_accvgpr_read_b32 %1, a139\n\tv_mul_f32 %1, %2, %1\n\tv_accvgpr_write_b32 a139, %1\n\t" "v_accvgpr_read_b32 %0, a140\n\tv_mul_f32 %0, %2, %0\n\tv_accvgpr_write_b32 a140, %0\n\t" "v_accvgpr_read_b32 %1, a141\n\tv_mul_f32 %1, %2, %1\n\tv_accvgpr_write_b32 a141, %1\n\t" "v_accvgpr_read_b32 %0, a142\n\tv_mul_f32 %0, %2, %0\n\tv_accvgpr_write_b32 a142, %0\n\t" "v_accvgpr_read_b32 %1, a143\n\tv_mul_f32 %1, %2, %1\n\tv_accvgpr_write_b32 a143, %1\n\t" "v_accvgpr_read_b32 %0, a144\n\tv_mul_f32 %0, %2, %0\n\tv_accvgpr_write_b32 a144, %0\n\t" "v_accvgpr_read_b32 %1, a145\n\tv_mul_f32 %1, %2, %1\n\tv_accvgpr_write_b32 a145, %1\n\t" "v_accvgpr_read_b32 %0, a146\n\tv_mul_f32 %0, %2, %0\n\tv_accvgpr_write_b32 a146, %0\n\t" "v_accvgpr_read_b32 %1, a147\n\tv_mul_f32 %1, %2, %1\n\tv_accvgpr_write_b32 a147, %1\n\t" "v_accvgpr_read_b32 %0, a148\n\tv_mul_f32 %0, %2, %0\n\tv_accvgpr_write_b32 a148, %0\n\t" "v_accvgpr_read_b32 %1, a149\n\tv_mul_f32 %1, %2, %1\n\tv_accvgpr_write_b32 a149, %1\n\t" "v_accvgpr_read_b32 %0, a150\n\tv_mul_f32 %0, %2, %0\n\tv_accvgpr_write_b32 a150, %0\n\t" "v_accvgpr_read_b32 %1, a151\n\tv_mul_f32 %1, %2, %1\n\tv_accvgpr_write_b32 a151, %1\n\t" "v_accvgpr_read_b32 %0, a152\n\tv_mul_f32 %0, %2, %0\n\tv_accvgpr_write_b32 a152, %0\n\t" "v_accvgpr_read_b32 %1, a153\n\tv_mul_f32 %1, %2, %1\n\tv_accvgpr_write_b32 a153, %1\n\t" "v_accvgpr_read_b32 %0, a154\n\tv_mul_f32 %0, %2, %0\n\tv_accvgpr_write_b32 a154, %0\n\t" "v_accvgpr_read_b32 %1, a155\n\tv_mul_f32 %1, %2, %1\n\tv_accvgpr_write_b32 a155, %1\n\t" "v_accvgpr_read_b32 %0, a156\n\tv_mul_f32 %0, %2, %0\n\tv_accvgpr_write_b32 a156, %0\n\t" "v_accvgpr_read_b32 %1, a157\n\tv_mul_f32 %1, %2, %1\n\tv_accvgpr_write_b32 a157, %1\n\t" "v_accvgpr_read_b32 %0, a158\n\tv_mul_f32 %0, %2, %0\n\tv_accvgpr_write_b32 a158, %0\n\t" "v_accvgpr_read_b32 %1, a159\n\tv_mul_f32 %1, %2, %1\n\tv_accvgpr_write_b32 a159, %1\n\t" "v_accvgpr_read_b32 %0, a160\n\tv_mul_f32 %0, %2, %0\n\tv_accvgpr_write_b32 a160, %0\n\t" "v_accvgpr_read_b32 %1, a161\n\tv_mul_f32 %1, %2, %1\n\tv_accvgpr_write_b32 a161, %1\n\t" "v_accvgpr_read_b32 %0, a162\n\tv_mul_f32 %0, %2, %0\n\tv_accvgpr_write_b32 a162, %0\n\t" "v_accvgpr_read_b32 %1, a163\n\tv_mul_f32 %1, %2, %1\n\tv_accvgpr_write_b32 a163, %1\n\t" "v_accvgpr_read_b32 %0, a164\n\tv_mul_f32 %0, %2, %0\n\tv_accvgpr_write_b32 a164, %0\n\t" "v_accvgpr_read_b32 %1, a165\n\tv_mul_f32 %1, %2, %1\n\tv_accvgpr_write_b32 a165, %1\n\t" "v_accvgpr_read_b32 %0, a166\n\tv_mul_f32 %0, %2, %0\n\tv_accvgpr_write_b32 a166, %0\n\t" "v_accvgpr_read_b32 %1, a167\n\tv_mul_f32 %1, %2, %1\n\tv_accvgpr_write_b32 a167, %1\n\t" "v_accvgpr_read_b32 %0, a168\n\tv_mul_f32 %0, %2, %0\n\tv_accvgpr_write_b32 a168, %0\n\t" "v_accvgpr_read_b32 %1, a169\n\tv_mul_f32 %1, %2, %1\n\tv_accvgpr_write_b32 a169, %1\n\t" "v_accvgpr_read_b32 %0, a170\n\tv_mul_f32 %0, %2, %0\n\tv_accvgpr_write_b32 a170, %0\n\t" "v_accvgpr_read_b32 %1, a171\n\tv_mul_f32 %1, %2, %1\n\tv_accvgpr_write_b32 a171, %1\n\t" "v_accvgpr_read_b32 %0, a172\n\tv_mul_f32 %0, %2, %0\n\tv_accvgpr_write_b32 a172, %0\n\t" "v_accvgpr_read_b32 %1, a173\n\tv_mul_f32 %1, %2, %1\n\tv_accvgpr_write_b32 a173, %1\n\t" "v_accvgpr_read_b32 %0, a174\n\tv_mul_f32 %0, %2, %0\n\tv_accvgpr_write_b32 a174, %0\n\t" "v_accvgpr_read_b32 %1, a175\n\tv_mul_f32 %1, %2, %1\n\tv_accvgpr_write_b32 a175, %1\n\t" "v_accvgpr_read_b32 %0, a176\n\tv_mul_f32 %0, %2, %0\n\tv_accvgpr_write_b32 a176, %0\n\t" "v_accvgpr_read_b32 %1, a177\n\tv_mul_f32 %1, %2, %1\n\tv_accvgpr_write_b32 a177, %1\n\t" "v_accvgpr_read_b32 %0, a178\n\tv_mul_f32 %0, %2, %0\n\tv_accvgpr_write_b32 a178, %0\n\t" "v_accvgpr_read_b32 %1, a179\n\tv_mul_f32 %1, %2, %1\n\tv_accvgpr_write_b32 a179, %1\n\t" "v_accvgpr_read_b32 %0, a180\n\tv_mul_f32 %0, %2, %0\n\tv_accvgpr_write_b32 a180, %0\n\t" "v_accvgpr_read_b32 %1, a181\n\tv_mul_f32 %1, %2, %1\n\tv_accvgpr_write_b32 a181, %1\n\t" "v_accvgpr_read_b32 %0, a182\n\tv_mul_f32 %0, %2, %0\n\tv_accvgpr_write_b32 a182, %0\n\t" "v_accvgpr_read_b32 %1, a183\n\tv_mul_f32 %1, %2, %1\n\tv_accvgpr_write_b32 a183, %1\n\t" "v_accvgpr_read_b32 %0, a184\n\tv_mul_f32 %0, %2, %0\n\tv_accvgpr_write_b32 a184, %0\n\t" "v_accvgpr_read_b32 %1, a185\n\tv_mul_f32 %1, %2, %1\n\tv_accvgpr_write_b32 a185, %1\n\t" "v_accvgpr_read_b32 %0, a186\n\tv_mul_f32 %0, %2, %0\n\tv_accvgpr_write_b32 a186, %0\n\t" "v_accvgpr_read_b32 %1, a187\n\tv_mul_f32 %1, %2, %1\n\tv_accvgpr_write_b32 a187, %1\n\t" "v_accvgpr_read_b32 %0, a188\n\tv_mul_f32 %0, %2, %0\n\tv_accvgpr_write_b32 a188, %0\n\t" "v_accvgpr_read_b32 %1, a189\n\tv_mul_f32 %1, %2, %1\n\tv_accvgpr_write_b32 a189, %1\n\t" "v_accvgpr_read_b32 %0, a190\n\tv_mul_f32 %0, %2, %0\n\tv_accvgpr_write_b32 a190, %0\n\t" "v_accvgpr_read_b32 %1, a191\n\tv_mul_f32 %1, %2, %1\n\tv_accvgpr_write_b32 a191, %1\n\t" : "=&v"(t0), "=&v"(t1) : "v"(alv[2]) : ACLOB); }
    asm volatile("s_nop 1" ::: "memory"); pend = 0;
  }
  SBAR();
  asm volatile("s_waitcnt vmcnt(8)\n\ts_barrier" ::: "memory");
  kf[0] = KRD(1, 0, 0); kf[1] = KRD(1, 0, 1); kf[2] = KRD(1, 0, 2); kf[3] = KRD(1, 0, 3); kf[4] = KRD(1, 0, 4); kf[5] = KRD(1, 0, 5);
  { MF_S0A(sB[0][0], kf[0], 192); kf[6] = KRD(1, 0, 6); SBAR(); }
  { MF_S0A(sB[1][0], kf[0], 224); DMA_V1(NT - 1, 1, 0); SBAR(); }
  { MF_S0V(sB[2][0], kf[0], qf2[0]); sA[2][2][0] = exp_s(ffma_s(sA[2][2][0], csc, nmc[2])); PIN(sA[2][2][0]); SBAR(); }
  { MF_SA(sB[0][0], kf[1], 196); kf[7] = KRD(1, 0, 7); SBAR(); }
  { MF_SA(sB[1][0], kf[1], 228); DMA_V1(NT - 1, 1, 1); SBAR(); }
  { MF_SV(sB[2][0], kf[1], qf2[1]); sA[2][2][1] = exp_s(ffma_s(sA[2][2][1], csc, nmc[2])); PIN(sA[2][2][1]); SBAR(); }
  { MF_SA(sB[0][0], kf[2], 200); kf[0] = KRD(1, 1, 0); SBAR(); }
  { MF_SA(sB[1][0], kf[2], 232); DMA_V1(NT - 1, 1, 2); SBAR(); }
  { MF_SV(sB[2][0], kf[2], qf2[2]); sA[2][2][2] = exp_s(ffma_s(sA[2][2][2], csc, nmc[2])); PIN(sA[2][2][2]); SBAR(); }
  { MF_SA(sB[0][0], kf[3], 204); kf[1] = KRD(1, 1, 1); SBAR(); }
  { MF_SA(sB[1][0], kf[3], 236); DMA_V1(NT - 1, 1, 3); SBAR(); }
  { MF_SV(sB[2][0], kf[3], qf2[3]); sA[2][2][3] = exp_s(ffma_s(sA[2][2][3], csc, nmc[2])); PIN(sA[2][2][3]); SBAR(); }
  { MF_SA(sB[0][0], kf[4], 208); kf[2] = KRD(1, 1, 2); SBAR(); }
  { MF_SA(sB[1][0], kf[4], 240); DMA_V1(NT - 1, 1, 4); SBAR(); }
  { MF_SV(sB[2][0], kf[4], qf2[4]); sA[2][3][0] = exp_s(ffma_s(sA[2][3][0], csc, nmc[2])); PIN(sA[2][3][0]); SBAR(); }
  { MF_SA(sB[0][0], kf[5], 212); kf[3] = KRD(1, 1, 3); SBAR(); }
  { MF_SA(sB[1][0], kf[5], 244); DMA_V1(NT - 1, 1, 5); SBAR(); }
  { MF_SV(sB[2][0], kf[5], qf2[5]); sA[2][3][1] = exp_s(ffma_s(sA[2][3][1], csc, nmc[2])); PIN(sA[2][3][1]); SBAR(); }
  { MF_SA(sB[0][0], kf[6], 216); kf[4] = KRD(1, 1, 4); SBAR(); }
  { MF_SA(sB[1][0], kf[6], 248); DMA_V1(NT - 1, 1, 6); SBAR(); }
  { MF_SV(sB[2][0], kf[6], qf2[6]); sA[2][3][2] = exp_s(ffma_s(sA[2][3][2], csc, nmc[2])); PIN(sA[2][3][2]); SBAR(); }
  { MF_SA(sB[0][0], kf[7], 220); kf[5] = KRD(1, 1, 5); SBAR(); }
  { MF_SA(sB[1][0], kf[7], 252); DMA_V1(NT - 1, 1, 7); SBAR(); }
  { MF_SV(sB[2][0], kf[7], qf2[7]); sA[2][3][3] = exp_s(ffma_s(sA[2][3][3], csc, nmc[2])); PIN(sA[2][3][3]); SBAR(); }
  { MF_S0A(sB[0][1], kf[0], 192); kf[6] = KRD(1, 1, 6); SBAR(); }
  { MF_S0A(sB[1][1], kf[0], 224); l[0] = fadd_s(l[0], sA[0][0][0]); PIN(l[0]); l[0] = fadd_s(l[0], sA[0][0][1]); PIN(l[0]); SBAR(); }
  { MF_S0V(sB[2][1], kf[0], qf2[0]); pw[0][0][0] = cvtpk(sA[0][0][0], sA[0][0][1]); PIN(pw[0][0][0]); SBAR(); }
  { MF_SA(sB[0][1], kf[1], 196); kf[7] = KRD(1, 1, 7); SBAR(); }
  { MF_SA(sB[1][1], kf[1], 228); l[0] = fadd_s(l[0], sA[0][0][2]); PIN(l[0]); l[0] = fadd_s(l[0], sA[0][0][3]); PIN(l[0]); SBAR(); }
  { MF_SV(sB[2][1], kf[1], qf2[1]); pw[0][0][1] = cvtpk(sA[0][0][2], sA[0][0][3]); PIN(pw[0][0][1]); SBAR(); }
  { MF_SA(sB[0][1], kf[2], 200); kf[0] = KRD(1, 2, 0); SBAR(); }
  { MF_SA(sB[1][1], kf[2], 232); l[0] = fadd_s(l[0], sA[0][1][0]); PIN(l[0]); l[0] = fadd_s(l[0], sA[0][1][1]); PIN(l[0]); SBAR(); }
  { MF_SV(sB[2][1], kf[2], qf2[2]); pw[0][0][2] = cvtpk(sA[0][1][0], sA[0][1][1]); PIN(pw[0][0][2]); SBAR(); }
  { MF_SA(sB[0][1], kf[3], 204); kf[1] = KRD(1, 2, 1); SBAR(); }
  { MF_SA(sB[1][1], kf[3], 236); l[0] = fadd_s(l[0], sA[0][1][2]); PIN(l[0]); l[0] = fadd_s(l[0], sA[0][1][3]); PIN(l[0]); SBAR(); }
  { MF_SV(sB[2][1], kf[3], qf2[3]); pw[0][0][3] = cvtpk(sA[0][1][2], sA[0][1][3]); PIN(pw[0][0][3]); SBAR(); }
  { MF_SA(sB[0][1], kf[4], 208); kf[2] = KRD(1, 2, 2); SBAR(); }
  { MF_SA(sB[1][1], kf[4], 240); l[0] = fadd_s(l[0], sA[0][2][0]); PIN(l[0]); l[0] = fadd_s(l[0], sA[0][2][1]); PIN(l[0]); SBAR(); }
  { MF_SV(sB[2][1], kf[4], qf2[4]); pw[0][1][0] = cvtpk(sA[0][2][0], sA[0][2][1]); PIN(pw[0][1][0]); SBAR(); }
  { MF_SA(sB[0][1], kf[5], 212); kf[3] = KRD(1, 2, 3); SBAR(); }
  { MF_SA(sB[1][1], kf[5], 244); l[0] = fadd_s(l[0], sA[0][2][2]); PIN(l[0]); l[0] = fadd_s(l[0], sA[0][2][3]); PIN(l[0]); SBAR(); }
  { MF_SV(sB[2][1], kf[5], qf2[5]); pw[0][1][1] = cvtpk(sA[0][2][2], sA[0][2][3]); PIN(pw[0][1][1]); SBAR(); }
  { MF_SA(sB[0][1], kf[6], 216); kf[4] = KRD(1, 2, 4); SBAR(); }
  { MF_SA(sB[1][1], kf[6], 248); l[0] = fadd_s(l[0], sA[0][3][0]); PIN(l[0]); l[0] = fadd_s(l[0], sA[0][3][1]); PIN(l[0]); SBAR(); }
  { MF_SV(sB[2][1], kf[6], qf2[6]); pw[0][1][2] = cvtpk(sA[0][3][0], sA[0][3][1]); PIN(pw[0][1][2]); SBAR(); }
  { MF_SA(sB[0][1], kf[7], 220); kf[5] = KRD(1, 2, 5); SBAR(); }
  { MF_SA(sB[1][1], kf[7], 252); l[0] = fadd_s(l[0], sA[0][3][2]); PIN(l[0]); l[0] = fadd_s(l[0], sA[0][3][3]); PIN(l[0]); SBAR(); }
  { MF_SV(sB[2][1], kf[7], qf2[7]); pw[0][1][3] = cvtpk(sA[0][3][2], sA[0][3][3]); PIN(pw[0][1][3]); SBAR(); }
  { MF_S0A(sB[0][2], kf[0], 192); kf[6] = KRD(1, 2, 6); SBAR(); }
  { MF_S0A(sB[1][2], kf[0], 224); l[1] = fadd_s(l[1], sA[1][0][0]); PIN(l[1]); l[1] = fadd_s(l[1], sA[1][0][1]); PIN(l[1]); SBAR(); }
  { MF_S0V(sB[2][2], kf[0], qf2[0]); pw[1][0][0] = cvtpk(sA[1][0][0], sA[1][0][1]); PIN(pw[1][0][0]); SBAR(); }
  { MF_SA(sB[0][2], kf[1], 196); kf[7] = KRD(1, 2, 7); SBAR(); }
  { MF_SA(sB[1][2], kf[1], 228); l[1] = fadd_s(l[1], sA[1][0][2]); PIN(l[1]); l[1] = fadd_s(l[1], sA[1][0][3]); PIN(l[1]); SBAR(); }
  { MF_SV(sB[2][2], kf[1], qf2[1]); pw[1][0][1] = cvtpk(sA[1][0][2], sA[1][0][3]); PIN(pw[1][0][1]); SBAR(); }
  { MF_SA(sB[0][2], kf[2], 200); kf[0] = KRD(1, 3, 0); SBAR(); }
  { MF_SA(sB[1][2], kf[2], 232); l[1] = fadd_s(l[1], sA[1][1][0]); PIN(l[1]); l[1] = fadd_s(l[1], sA[1][1][1]); PIN(l[1]); SBAR(); }
  { MF_SV(sB[2][2], kf[2], qf2[2]); pw[1][0][2] = cvtpk(sA[1][1][0], sA[1][1][1]); PIN(pw[1][0][2]); SBAR(); }
  { MF_SA(sB[0][2], kf[3], 204); kf[1] = KRD(1, 3, 1); SBAR(); }
  { MF_SA(sB[1][2], kf[3], 236); l[1] = fadd_s(l[1], sA[1][1][2]); PIN(l[1]); l[1] = fadd_s(l[1], sA[1][1][3]); PIN(l[1]); SBAR(); }
  { MF_SV(sB[2][2], kf[3], qf2[3]); pw[1][0][3] = cvtpk(sA[1][1][2], sA[1][1][3]); PIN(pw[1][0][3]); SBAR(); }
  { MF_SA(sB[0][2], kf[4], 208); kf[2] = KRD(1, 3, 2); SBAR(); }
  { MF_SA(sB[1][2], kf[4], 240); l[1] = fadd_s(l[1], sA[1][2][0]); PIN(l[1]); l[1] = fadd_s(l[1], sA[1][2][1]); PIN(l[1]); SBAR(); }
  { MF_SV(sB[2][2], kf[4], qf2[4]); pw[1][1][0] = cvtpk(sA[1][2][0], sA[1][2][1]); PIN(pw[1][1][0]); SBAR(); }
  { MF_SA(sB[0][2], kf[5], 212); kf[3] = KRD(1, 3, 3); SBAR(); }
  { MF_SA(sB[1][2], kf[5], 244); l[1] = fadd_s(l[1], sA[1][2][2]); PIN(l[1]); l[1] = fadd_s(l[1], sA[1][2][3]); PIN(l[1]); SBAR(); }
  { MF_SV(sB[2][2], kf[5], qf2[5]); pw[1][1][1] = cvtpk(sA[1][2][2], sA[1][2][3]); PIN(pw[1][1][1]); SBAR(); }
  { MF_SA(sB[0][2], kf[6], 216); kf[4] = KRD(1, 3, 4); SBAR(); }
  { MF_SA(sB[1][2], kf[6], 248); l[1] = fadd_s(l[1], sA[1][3][0]); PIN(l[1]); l[1] = fadd_s(l[1], sA[1][3][1]); PIN(l[1]); SBAR(); }
  { MF_SV(sB[2][2], kf[6], qf2[6]); pw[1][1][2] = cvtpk(sA[1][3][0], sA[1][3][1]); PIN(pw[1][1][2]); SBAR(); }
  { MF_SA(sB[0][2], kf[7], 220); kf[5] = KRD(1, 3, 5); SBAR(); }
  { MF_SA(sB[1][2], kf[7], 252); l[1] = fadd_s(l[1], sA[1][3][2]); PIN(l[1]); l[1] = fadd_s(l[1], sA[1][3][3]); PIN(l[1]); SBAR(); }
  { MF_SV(sB[2][2], kf[7], qf2[7]); pw[1][1][3] = cvtpk(sA[1][3][2], sA[1][3][3]); PIN(pw[1][1][3]); SBAR(); }
  { MF_S0A(sB[0][3], kf[0], 192); kf[6] = KRD(1, 3, 6); SBAR(); }
  { MF_S0A(sB[1][3], kf[0], 224); l[2] = fadd_s(l[2], sA[2][0][0]); PIN(l[2]); l[2] = fadd_s(l[2], sA[2][0][1]); PIN(l[2]); SBAR(); }
  { MF_S0V(sB[2][3], kf[0], qf2[0]); pw[2][0][0] = cvtpk(sA[2][0][0], sA[2][0][1]); PIN(pw[2][0][0]); SBAR(); }
  { MF_SA(sB[0][3], kf[1], 196); kf[7] = KRD(1, 3, 7); SBAR(); }
  { MF_SA(sB[1][3], kf[1], 228); l[2] = fadd_s(l[2], sA[2][0][2]); PIN(l[2]); l[2] = fadd_s(l[2], sA[2][0][3]); PIN(l[2]); SBAR(); }
  { MF_SV(sB[2][3], kf[1], qf2[1]); pw[2][0][1] = cvtpk(sA[2][0][2], sA[2][0][3]); PIN(pw[2][0][1]); SBAR(); }
  { MF_SA(sB[0][3], kf[2], 200); vf[0] = VRD(0, 0, 0); SBAR(); }
  { MF_SA(sB[1][3], kf[2], 232); l[2] = fadd_s(l[2], sA[2][1][0]); PIN(l[2]); l[2] = fadd_s(l[2], sA[2][1][1]); PIN(l[2]); SBAR(); }
  { MF_SV(sB[2][3], kf[2], qf2[2]); pw[2][0][2] = cvtpk(sA[2][1][0], sA[2][1][1]); PIN(pw[2][0][2]); SBAR(); }
  { MF_SA(sB[0][3], kf[3], 204); vf[1] = VRD(0, 0, 1); SBAR(); }
  { MF_SA(sB[1][3], kf[3], 236); l[2] = fadd_s(l[2], sA[2][1][2]); PIN(l[2]); l[2] = fadd_s(l[2], sA[2][1][3]); PIN(l[2]); SBAR(); }
  { MF_SV(sB[2][3], kf[3], qf2[3]); pw[2][0][3] = cvtpk(sA[2][1][2], sA[2][1][3]); PIN(pw[2][0][3]); SBAR(); }
  { MF_SA(sB[0][3], kf[4], 208); vf[2] = VRD(0, 0, 2); SBAR(); }
  { MF_SA(sB[1][3], kf[4], 240); l[2] = fadd_s(l[2], sA[2][2][0]); PIN(l[2]); l[2] = fadd_s(l[2], sA[2][2][1]); PIN(l[2]); SBAR(); }
  { MF_SV(sB[2][3], kf[4], qf2[4]); pw[2][1][0] = cvtpk(sA[2][2][0], sA[2][2][1]); PIN(pw[2][1][0]); SBAR(); }
  { MF_SA(sB[0][3], kf[5], 212); vf[3] = VRD(0, 0, 3); SBAR(); }
  { MF_SA(sB[1][3], kf[5], 244); l[2] = fadd_s(l[2], sA[2][2][2]); PIN(l[2]); l[2] = fadd_s(l[2], sA[2][2][3]); PIN(l[2]); SBAR(); }
  { MF_SV(sB[2][3], kf[5], qf2[5]); pw[2][1][1] = cvtpk(sA[2][2][2], sA[2][2][3]); PIN(pw[2][1][1]); SBAR(); }
  { MF_SA(sB[0][3], kf[6], 216); vf[4] = VRD(0, 0, 4); SBAR(); }
  { MF_SA(sB[1][3], kf[6], 248); l[2] = fadd_s(l[2], sA[2][3][0]); PIN(l[2]); l[2] = fadd_s(l[2], sA[2][3][1]); PIN(l[2]); SBAR(); }
  { MF_SV(sB[2][3], kf[6], qf2[6]); pw[2][1][2] = cvtpk(sA[2][3][0], sA[2][3][1]); PIN(pw[2][1][2]); SBAR(); }
  { MF_SA(sB[0][3], kf[7], 220); vf[5] = VRD(0, 0, 5); SBAR(); }
  { MF_SA(sB[1][3], kf[7], 252); l[2] = fadd_s(l[2], sA[2][3][2]); PIN(l[2]); l[2] = fadd_s(l[2], sA[2][3][3]); PIN(l[2]); SBAR(); }
  { MF_SV(sB[2][3], kf[7], qf2[7]); pw[2][1][3] = cvtpk(sA[2][3][2], sA[2][3][3]); PIN(pw[2][1][3]); SBAR(); }
  _Pragma("unroll") for (int sb = 0; sb < 3; ++sb) { pf[sb][0] = __builtin_bit_cast(bf16x8, pw[sb][0]); pf[sb][1] = __builtin_bit_cast(bf16x8, pw[sb][1]); PIN(pf[sb][0]); PIN(pf[sb][1]); }
  asm volatile("s_nop 1" ::: "memory"); SBAR();
  asm volatile("s_waitcnt vmcnt(8)\n\ts_barrier" ::: "memory");
  { MF_O(0, vf[0], pf[0][0]); vf[6] = VRD(0, 0, 6); SBAR(); }
  { MF_O(64, vf[0], pf[1][0]); mx[0] = max2f(sB[0][0][0], sB[0][0][1]); PIN(mx[0]); SBAR(); }
  { MF_O(128, vf[0], pf[2][0]); mx[1] = max2f(sB[1][0][0], sB[1][0][1]); PIN(mx[1]); SBAR(); }
  { MF_O(4, vf[1], pf[0][0]); vf[7] = VRD(0, 0, 7); SBAR(); }
  { MF_O(68, vf[1], pf[1][0]); mx[2] = max2f(sB[2][0][0], sB[2][0][1]); PIN(mx[2]); SBAR(); }
  { MF_O(132, vf[1], pf[2][0]); mx[0] = max3f(mx[0], sB[0][0][2], sB[0][0][3]); PIN(mx[0]); SBAR(); }
  { MF_O(8, vf[2], pf[0][0]); vf[0] = VRD(0, 0, 8); SBAR(); }
  { MF_O(72, vf[2], pf[1][0]); mx[1] = max3f(mx[1], sB[1][0][2], sB[1][0][3]); PIN(mx[1]); SBAR(); }
  { MF_O(136, vf[2], pf[2][0]); mx[2] = max3f(mx[2], sB[2][0][2], sB[2][0][3]); PIN(mx[2]); SBAR(); }
  { MF_O(12, vf[3], pf[0][0]); vf[1] = VRD(0, 0, 9); SBAR(); }
  { MF_O(76, vf[3], pf[1][0]); mx[0] = max3f(mx[0], sB[0][1][0], sB[0][1][1]); PIN(mx[0]); SBAR(); }
  { MF_O(140, vf[3], pf[2][0]); mx[1] = max3f(mx[1], sB[1][1][0], sB[1][1][1]); PIN(mx[1]); SBAR(); }
  { MF_O(16, vf[4], pf[0][0]); vf[2] = VRD(0, 0, 10); SBAR(); }
  { MF_O(80, vf[4], pf[1][0]); mx[2] = max3f(mx[2], sB[2][1][0], sB[2][1][1]); PIN(mx[2]); SBAR(); }
  { MF_O(144, vf[4], pf[2][0]); mx[0] = max3f(mx[0], sB[0][1][2], sB[0][1][3]); PIN(mx[0]); SBAR(); }
  { MF_O(20, vf[5], pf[0][0]); vf[3] = VRD(0, 0, 11); SBAR(); }
  { MF_O(84, vf[5], pf[1][0]); mx[1] = max3f(mx[1], sB[1][1][2], sB[1][1][3]); PIN(mx[1]); SBAR(); }
  { MF_O(148, vf[5], pf[2][0]); mx[2] = max3f(mx[2], sB[2][1][2], sB[2][1][3]); PIN(mx[2]); SBAR(); }
  { MF_O(24, vf[6], pf[0][0]); vf[4] = VRD(0, 0, 12); SBAR(); }
  { MF_O(88, vf[6], pf[1][0]); mx[0] = max3f(mx[0], sB[0][2][0], sB[0][2][1]); PIN(mx[0]); SBAR(); }
  { MF_O(152, vf[6], pf[2][0]); mx[1] = max3f(mx[1], sB[1][2][0], sB[1][2][1]); PIN(mx[1]); SBAR(); }
  { MF_O(28, vf[7], pf[0][0]); vf[5] = VRD(0, 0, 13); SBAR(); }
  { MF_O(92, vf[7], pf[1][0]); mx[2] = max3f(mx[2], sB[2][2][0], sB[2][2][1]); PIN(mx[2]); SBAR(); }
  { MF_O(156, vf[7], pf[2][0]); mx[0] = max3f(mx[0], sB[0][2][2], sB[0][2][3]); PIN(mx[0]); SBAR(); }
  { MF_O(32, vf[0], pf[0][0]); vf[6] = VRD(0, 0, 14); SBAR(); }
  { MF_O(96, vf[0], pf[1][0]); mx[1] = max3f(mx[1], sB[1][2][2], sB[1][2][3]); PIN(mx[1]); SBAR(); }
  { MF_O(160, vf[0], pf[2][0]); mx[2] = max3f(mx[2], sB[2][2][2], sB[2][2][3]); PIN(mx[2]); SBAR(); }
  { MF_O(36, vf[1], pf[0][0]); vf[7] = VRD(0, 0, 15); SBAR(); }
  { MF_O(100, vf[1], pf[1][0]); mx[0] = max3f(mx[0], sB[0][3][0], sB[0][3][1]); PIN(mx[0]); SBAR(); }
  { MF_O(164, vf[1], pf[2][0]); mx[1] = max3f(mx[1], sB[1][3][0], sB[1][3][1]); PIN(mx[1]); SBAR(); }
  { MF_O(40, vf[2], pf[0][0]); vf[0] = VRD(0, 1, 0); SBAR(); }
  { MF_O(104, vf[2], pf[1][0]); mx[2] = max3f(mx[2], sB[2][3][0], sB[2][3][1]); PIN(mx[2]); SBAR(); }
  { MF_O(168, vf[2], pf[2][0]); mx[0] = max3f(mx[0], sB[0][3][2], sB[0][3][3]); PIN(mx[0]); SBAR(); }
  { MF_O(44, vf[3], pf[0][0]); vf[1] = VRD(0, 1, 1); SBAR(); }
  { MF_O(108, vf[3], pf[1][0]); mx[1] = max3f(mx[1], sB[1][3][2], sB[1][3][3]); PIN(mx[1]); SBAR(); }
  { MF_O(172, vf[3], pf[2][0]); mx[2] = max3f(mx[2], sB[2][3][2], sB[2][3][3]); PIN(mx[2]); SBAR(); }
  { MF_O(48, vf[4], pf[0][0]); vf[2] = VRD(0, 1, 2); SBAR(); }
  { MF_O(112, vf[4], pf[1][0]); mx[0] = xmax16(mx[0]); PIN(mx[0]); SBAR(); }
  { MF_O(176, vf[4], pf[2][0]); mx[1] = xmax16(mx[1]); PIN(mx[1]); SBAR(); }
  { MF_O(52, vf[5], pf[0][0]); vf[3] = VRD(0, 1, 3); SBAR(); }
  { MF_O(116, vf[5], pf[1][0]); mx[2] = xmax16(mx[2]); PIN(mx[2]); SBAR(); }
  { MF_O(180, vf[5], pf[2][0]); mx[0] = xmax32(mx[0]); PIN(mx[0]); SBAR(); }
  { MF_O(56, vf[6], pf[0][0]); vf[4] = VRD(0, 1, 4); SBAR(); }
  { MF_O(120, vf[6], pf[1][0]); mx[1] = xmax32(mx[1]); PIN(mx[1]); SBAR(); }
  { MF_O(184, vf[6], pf[2][0]); mx[2] = xmax32(mx[2]); PIN(mx[2]); SBAR(); }
  { MF_O(60, vf[7], pf[0][0]); vf[5] = VRD(0, 1, 5); SBAR(); }
  {
    const bool grow = (mx[0] - m[0] > THR) | (mx[1] - m[1] > THR) | (mx[2] - m[2] > THR);
    if (__builtin_expect(__any((int)grow), 0)) {
#pragma unroll
      for (int sb = 0; sb < 3; ++sb) { const float mn = fmaxf(m[sb], mx[sb]);
        const float al = __builtin_amdgcn_exp2f((m[sb] - mn) * CSC); m[sb] = mn; nmc[sb] = -mn * CSC; l[sb] *= al; alv[sb] = al; }
      pend = 1;
    }
    SBAR();
  }
  { MF_O(124, vf[7], pf[1][0]); sB[0][0][0] = exp_s(ffma_s(sB[0][0][0], csc, nmc[0])); PIN(sB[0][0][0]); SBAR(); }
  { MF_O(188, vf[7], pf[2][0]); sB[0][0][1] = exp_s(ffma_s(sB[0][0][1], csc, nmc[0])); PIN(sB[0][0][1]); SBAR(); }
  { MF_O(0, vf[0], pf[0][1]); vf[6] = VRD(0, 1, 6); sB[0][0][2] = exp_s(ffma_s(sB[0][0][2], csc, nmc[0])); PIN(sB[0][0][2]); SBAR(); }
  { MF_O(64, vf[0], pf[1][1]); sB[0][0][3] = exp_s(ffma_s(sB[0][0][3], csc, nmc[0])); PIN(sB[0][0][3]); SBAR(); }
  { MF_O(128, vf[0], pf[2][1]); sB[0][1][0] = exp_s(ffma_s(sB[0][1][0], csc, nmc[0])); PIN(sB[0][1][0]); SBAR(); }
  { MF_O(4, vf[1], pf[0][1]); vf[7] = VRD(0, 1, 7); sB[0][1][1] = exp_s(ffma_s(sB[0][1][1], csc, nmc[0])); PIN(sB[0][1][1]); SBAR(); }
  { MF_O(68, vf[1], pf[1][1]); sB[0][1][2] = exp_s(ffma_s(sB[0][1][2], csc, nmc[0])); PIN(sB[0][1][2]); SBAR(); }
  { MF_O(132, vf[1], pf[2][1]); sB[0][1][3] = exp_s(ffma_s(sB[0][1][3], csc, nmc[0])); PIN(sB[0][1][3]); SBAR(); }
  { MF_O(8, vf[2], pf[0][1]); vf[0] = VRD(0, 1, 8); sB[0][2][0] = exp_s(ffma_s(sB[0][2][0], csc, nmc[0])); PIN(sB[0][2][0]); SBAR(); }
  { MF_O(72, vf[2], pf[1][1]); sB[0][2][1] = exp_s(ffma_s(sB[0][2][1], csc, nmc[0])); PIN(sB[0][2][1]); SBAR(); }
  { MF_O(136, vf[2], pf[2][1]); sB[0][2][2] = exp_s(ffma_s(sB[0][2][2], csc, nmc[0])); PIN(sB[0][2][2]); SBAR(); }
  { MF_O(12, vf[3], pf[0][1]); vf[1] = VRD(0, 1, 9); sB[0][2][3] = exp_s(ffma_s(sB[0][2][3], csc, nmc[0])); PIN(sB[0][2][3]); SBAR(); }
  { MF_O(76, vf[3], pf[1][1]); sB[0][3][0] = exp_s(ffma_s(sB[0][3][0], csc, nmc[0])); PIN(sB[0][3][0]); SBAR(); }
  { MF_O(140, vf[3], pf[2][1]); sB[0][3][1] = exp_s(ffma_s(sB[0][3][1], csc, nmc[0])); PIN(sB[0][3][1]); SBAR(); }
  { MF_O(16, vf[4], pf[0][1]); vf[2] = VRD(0, 1, 10); sB[0][3][2] = exp_s(ffma_s(sB[0][3][2], csc, nmc[0])); PIN(sB[0][3][2]); SBAR(); }
  { MF_O(80, vf[4], pf[1][1]); sB[0][3][3] = exp_s(ffma_s(sB[0][3][3], csc, nmc[0])); PIN(sB[0][3][3]); SBAR(); }
  { MF_O(144, vf[4], pf[2][1]); sB[1][0][0] = exp_s(ffma_s(sB[1][0][0], csc, nmc[1])); PIN(sB[1][0][0]); SBAR(); }
  { MF_O(20, vf[5], pf[0][1]); vf[3] = VRD(0, 1, 11); sB[1][0][1] = exp_s(ffma_s(sB[1][0][1], csc, nmc[1])); PIN(sB[1][0][1]); SBAR(); }
  { MF_O(84, vf[5], pf[1][1]); sB[1][0][2] = exp_s(ffma_s(sB[1][0][2], csc, nmc[1])); PIN(sB[1][0][2]); SBAR(); }
  { MF_O(148, vf[5], pf[2][1]); sB[1][0][3] = exp_s(ffma_s(sB[1][0][3], csc, nmc[1])); PIN(sB[1][0][3]); SBAR(); }
  { MF_O(24, vf[6], pf[0][1]); vf[4] = VRD(0, 1, 12); SBAR(); }
  { MF_O(88, vf[6], pf[1][1]); sB[1][1][0] = exp_s(ffma_s(sB[1][1][0], csc, nmc[1])); PIN(sB[1][1][0]); SBAR(); }
  { MF_O(152, vf[6], pf[2][1]); sB[1][1][1] = exp_s(ffma_s(sB[1][1][1], csc, nmc[1])); PIN(sB[1][1][1]); SBAR(); }
  { MF_O(28, vf[7], pf[0][1]); vf[5] = VRD(0, 1, 13); SBAR(); }
  { MF_O(92, vf[7], pf[1][1]); sB[1][1][2] = exp_s(ffma_s(sB[1][1][2], csc, nmc[1])); PIN(sB[1][1][2]); SBAR(); }
  { MF_O(156, vf[7], pf[2][1]); sB[1][1][3] = exp_s(ffma_s(sB[1][1][3], csc, nmc[1])); PIN(sB[1][1][3]); SBAR(); }
  { MF_O(32, vf[0], pf[0][1]); vf[6] = VRD(0, 1, 14); SBAR(); }
  { MF_O(96, vf[0], pf[1][1]); sB[1][2][0] = exp_s(ffma_s(sB[1][2][0], csc, nmc[1])); PIN(sB[1][2][0]); SBAR(); }
  { MF_O(160, vf[0], pf[2][1]); sB[1][2][1] = exp_s(ffma_s(sB[1][2][1], csc, nmc[1])); PIN(sB[1][2][1]); SBAR(); }
  { MF_O(36, vf[1], pf[0][1]); vf[7] = VRD(0, 1, 15); SBAR(); }
  { MF_O(100, vf[1], pf[1][1]); sB[1][2][2] = exp_s(ffma_s(sB[1][2][2], csc, nmc[1])); PIN(sB[1][2][2]); SBAR(); }
  { MF_O(164, vf[1], pf[2][1]); sB[1][2][3] = exp_s(ffma_s(sB[1][2][3], csc, nmc[1])); PIN(sB[1][2][3]); SBAR(); }
  { MF_O(40, vf[2], pf[0][1]);  SBAR(); }
  { MF_O(104, vf[2], pf[1][1]); sB[1][3][0] = exp_s(ffma_s(sB[1][3][0], csc, nmc[1])); PIN(sB[1][3][0]); SBAR(); }
  { MF_O(168, vf[2], pf[2][1]); sB[1][3][1] = exp_s(ffma_s(sB[1][3][1], csc, nmc[1])); PIN(sB[1][3][1]); SBAR(); }
  { MF_O(44, vf[3], pf[0][1]);  SBAR(); }
  { MF_O(108, vf[3], pf[1][1]); sB[1][3][2] = exp_s(ffma_s(sB[1][3][2], csc, nmc[1])); PIN(sB[1][3][2]); SBAR(); }
  { MF_O(172, vf[3], pf[2][1]); sB[1][3][3] = exp_s(ffma_s(sB[1][3][3], csc, nmc[1])); PIN(sB[1][3][3]); SBAR(); }
  { MF_O(48, vf[4], pf[0][1]);  SBAR(); }
  { MF_O(112, vf[4], pf[1][1]); sB[2][0][0] = exp_s(ffma_s(sB[2][0][0], csc, nmc[2])); PIN(sB[2][0][0]); SBAR(); }
  { MF_O(176, vf[4], pf[2][1]); sB[2][0][1] = exp_s(ffma_s(sB[2][0][1], csc, nmc[2])); PIN(sB[2][0][1]); SBAR(); }
  { MF_O(52, vf[5], pf[0][1]);  SBAR(); }
  { MF_O(116, vf[5], pf[1][1]); sB[2][0][2] = exp_s(ffma_s(sB[2][0][2], csc, nmc[2])); PIN(sB[2][0][2]); SBAR(); }
  { MF_O(180, vf[5], pf[2][1]); sB[2][0][3] = exp_s(ffma_s(sB[2][0][3], csc, nmc[2])); PIN(sB[2][0][3]); SBAR(); }
  { MF_O(56, vf[6], pf[0][1]);  SBAR(); }
  { MF_O(120, vf[6], pf[1][1]); sB[2][1][0] = exp_s(ffma_s(sB[2][1][0], csc, nmc[2])); PIN(sB[2][1][0]); SBAR(); }
  { MF_O(184, vf[6], pf[2][1]); sB[2][1][1] = exp_s(ffma_s(sB[2][1][1], csc, nmc[2])); PIN(sB[2][1][1]); SBAR(); }
  { MF_O(60, vf[7], pf[0][1]);  SBAR(); }
  { MF_O(124, vf[7], pf[1][1]); sB[2][1][2] = exp_s(ffma_s(sB[2][1][2], csc, nmc[2])); PIN(sB[2][1][2]); SBAR(); }
  { MF_O(188, vf[7], pf[2][1]); sB[2][1][3] = exp_s(ffma_s(sB[2][1][3], csc, nmc[2])); PIN(sB[2][1][3]); SBAR(); }
  if (__builtin_expect(pend, 0)) {
    asm volatile("s_nop 7\n\ts_nop 7" ::: "memory");
      { float t0, t1; asm volatile("v_accvgpr_read_b32 %0, a0\n\tv_mul_f32 %0, %2, %0\n\tv_accvgpr_write_b32 a0, %0\n\t" "v_accvgpr_read_b32 %1, a1\n\tv_mul_f32 %1, %2, %1\n\tv_accvgpr_write_b32 a1, %1\n\t" "v_accvgpr_read_b32 %0, a2\n\tv_mul_f32 %0, %2, %0\n\tv_accvgpr_write_b32 a2, %0\n\t" "v_accvgpr_read_b32 %1, a3\n\tv_mul_f32 %1, %2, %1\n\tv_accvgpr_write_b32 a3, %1\n\t" "v_accvgpr_read_b32 %0, a4\n\tv_mul_f32 %0, %2, %0\n\tv_accvgpr_write_b32 a4, %0\n\t" "v_accvgpr_read_b32 %1, a5\n\tv_mul_f32 %1, %2, %1\n\tv_accvgpr_write_b32 a5, %1\n\t" "v_accvgpr_read_b32 %0, a6\n\tv_mul_f32 %0, %2, %0\n\tv_accvgpr_write_b32 a6, %0\n\t" "v_accvgpr_read_b32 %1, a7\n\tv_mul_f32 %1, %2, %1\n\tv_accvgpr_write_b32 a7, %1\n\t" "v_accvgpr_read_b32 %0, a8\n\tv_mul_f32 %0, %2, %0\n\tv_accvgpr_write_b32 a8, %0\n\t" "v_accvgpr_read_b32 %1, a9\n\tv_mul_f32 %1, %2, %1\n\tv_accvgpr_write_b32 a9, %1\n\t" "v_accvgpr_read_b32 %0, a10\n\tv_mul_f32 %0, %2, %0\n\tv_accvgpr_write_b32 a10, %0\n\t" "v_accvgpr_read_b32 %1, a11\n\tv_mul_f32 %1, %2, %1\n\tv_accvgpr_write_b32 a11, %1\n\t" "v_accvgpr_read_b32 %0, a12\n\tv_mul_f32 %0, %2, %0\n\tv_accvgpr_write_b32 a12, %0\n\t" "v_accvgpr_read_b32 %1, a13\n\tv_mul_f32 %1, %2, %1\n\tv_accvgpr_write_b32 a13, %1\n\t" "v_accvgpr_read_b32 %0, a14\n\tv_mul_f32 %0, %2, %0\n\tv_accvgpr_write_b32 a14, %0\n\t" "v_accvgpr_read_b32 %1, a15\n\tv_mul_f32 %1, %2, %1\n\tv_accvgpr_write_b32 a15, %1\n\t" "v_accvgpr_read_b32 %0, a16\n\tv_mul_f32 %0, %2, %0\n\tv_accvgpr_write_b32 a16, %0\n\t" "v_accvgpr_read_b32 %1, a17\n\tv_mul_f32 %1, %2, %1\n\tv_accvgpr_write_b32 a17, %1\n\t" "v_accvgpr_read_b32 %0, a18\n\tv_mul_f32 %0, %2, %0\n\tv_accvgpr_write_b32 a18, %0\n\t" "v_accvgpr_read_b32 %1, a19\n\tv_mul_f32 %1, %2, %1\n\tv_accvgpr_write_b32 a19, %1\n\t" "v_accvgpr_read_b32 %0, a20\n\tv_mul_f32 %0, %2, %0\n\tv_accvgpr_write_b32 a20, %0\n\t" "v_accvgpr_read_b32 %1, a21\n\tv_mul_f32 %1, %2, %1\n\tv_accvgpr_write_b32 a21, %1\n\t" "v_accvgpr_read_b32 %0, a22\n\tv_mul_f32 %0, %2, %0\n\tv_accvgpr_write_b32 a22, %0\n\t" "v_accvgpr_read_b32 %1, a23\n\tv_mul_f32 %1, %2, %1\n\tv_accvgpr_write_b32 a23, %1\n\t" "v_accvgpr_read_b32 %0, a24\n\tv_mul_f32 %0, %2, %0\n\tv_accvgpr_write_b32 a24, %0\n\t" "v_accvgpr_read_b32 %1, a25\n\tv_mul_f32 %1, %2, %1\n\tv_accvgpr_write_b32 a25, %1\n\t" "v_accvgpr_read_b32 %0, a26\n\tv_mul_f32 %0, %2, %0\n\tv_accvgpr_write_b32 a26, %0\n\t" "v_accvgpr_read_b32 %1, a27\n\tv_mul_f32 %1, %2, %1\n\tv_accvgpr_write_b32 a27, %1\n\t" "v_accvgpr_read_b32 %0, a28\n\tv_mul_f32 %0, %2, %0\n\tv_accvgpr_write_b32 a28, %0\n\t" "v_accvgpr_read_b32 %1, a29\n\tv_mul_f32 %1, %2, %1\n\tv_accvgpr_write_b32 a29, %1\n\t" "v_accvgpr_read_b32 %0, a30\n\tv_mul_f32 %0, %2, %0\n\tv_accvgpr_write_b32 a30, %0\n\t" "v_accvgpr_read_b32 %1, a31\n\tv_mul_f32 %1, %2, %1\n\tv_accvgpr_write_b32 a31, %1\n\t" "v_accvgpr_read_b32 %0, a32\n\tv_mul_f32 %0, %2, %0\n\tv_accvgpr_write_b32 a32, %0\n\t" "v_accvgpr_read_b32 %1, a33\n\tv_mul_f32 %1, %2, %1\n\tv_accvgpr_write_b32 a33, %1\n\t" "v_accvgpr_read_b32 %0, a34\n\tv_mul_f32 %0, %2, %0\n\tv_accvgpr_write_b32 a34, %0\n\t" "v_accvgpr_read_b32 %1, a35\n\tv_mul_f32 %1, %2, %1\n\tv_accvgpr_write_b32 a35, %1\n\t" "v_accvgpr_read_b32 %0, a36\n\tv_mul_f32 %0, %2, %0\n\tv_accvgpr_write_b32 a36, %0\n\t" "v_accvgpr_read_b32 %1, a37\n\tv_mul_f32 %1, %2, %1\n\tv_accvgpr_write_b32 a37, %1\n\t" "v_accvgpr_read_b32 %0, a38\n\tv_mul_f32 %0, %2, %0\n\tv_accvgpr_write_b32 a38, %0\n\t" "v_accvgpr_read_b32 %1, a39\n\tv_mul_f32 %1, %2, %1\n\tv_accvgpr_write_b32 a39, %1\n\t" "v_accvgpr_read_b32 %0, a40\n\tv_mul_f32 %0, %2, %0\n\tv_accvgpr_write_b32 a40, %0\n\t" "v_accvgpr_read_b32 %1, a41\n\tv_mul_f32 %1, %2, %1\n\tv_accvgpr_write_b32 a41, %1\n\t" "v_accvgpr_read_b32 %0, a42\n\tv_mul_f32 %0, %2, %0\n\tv_accvgpr_write_b32 a42, %0\n\t" "v_accvgpr_read_b32 %1, a43\n\tv_mul_f32 %1, %2, %1\n\tv_accvgpr_write_b32 a43, %1\n\t" "v_accvgpr_read_b32 %0, a44\n\tv_mul_f32 %0, %2, %0\n\tv_accvgpr_write_b32 a44, %0\n\t" "v_accvgpr_read_b32 %1, a45\n\tv_mul_f32 %1, %2, %1\n\tv_accvgpr_write_b32 a45, %1\n\t" "v_accvgpr_read_b32 %0, a46\n\tv_mul_f32 %0, %2, %0\n\tv_accvgpr_write_b32 a46, %0\n\t" "v_accvgpr_read_b32 %1, a47\n\tv_mul_f32 %1, %2, %1\n\tv_accvgpr_write_b32 a47, %1\n\t" "v_accvgpr_read_b32 %0, a48\n\tv_mul_f32 %0, %2, %0\n\tv_accvgpr_write_b32 a48, %0\n\t" "v_accvgpr_read_b32 %1, a49\n\tv_mul_f32 %1, %2, %1\n\tv_accvgpr_write_b32 a49, %1\n\t" "v_accvgpr_read_b32 %0, a50\n\tv_mul_f32 %0, %2, %0\n\tv_accvgpr_write_b32 a50, %0\n\t" "v_accvgpr_read_b32 %1, a51\n\tv_mul_f32 %1, %2, %1\n\tv_accvgpr_write_b32 a51, %1\n\t" "v_accvgpr_read_b32 %0, a52\n\tv_mul_f32 %0, %2, %0\n\tv_accvgpr_write_b32 a52, %0\n\t" "v_accvgpr_read_b32 %1, a53\n\tv_mul_f32 %1, %2, %1\n\tv_accvgpr_write_b32 a53, %1\n\t" "v_accvgpr_read_b32 %0, a54\n\tv_mul_f32 %0, %2, %0\n\tv_accvgpr_write_b32 a54, %0\n\t" "v_accvgpr_read_b32 %1, a55\n\tv_mul_f32 %1, %2, %1\n\tv_accvgpr_write_b32 a55, %1\n\t" "v_accvgpr_read_b32 %0, a56\n\tv_mul_f32 %0, %2, %0\n\tv_accvgpr_write_b32 a56, %0\n\t" "v_accvgpr_read_b32 %1, a57\n\tv_mul_f32 %1, %2, %1\n\tv_accvgpr_write_b32 a57, %1\n\t" "v_accvgpr_read_b32 %0, a58\n\tv_mul_f32 %0, %2, %0\n\tv_accvgpr_write_b32 a58, %0\n\t" "v_accvgpr_read_b32 %1, a59\n\tv_mul_f32 %1, %2, %1\n\tv_accvgpr_write_b32 a59, %1\n\t" "v_accvgpr_read_b32 %0, a60\n\tv_mul_f32 %0, %2, %0\n\tv_accvgpr_write_b32 a60, %0\n\t" "v_accvgpr_read_b32 %1, a61\n\tv_mul_f32 %1, %2, %1\n\tv_accvgpr_write_b32 a61, %1\n\t" "v_accvgpr_read_b32 %0, a62\n\tv_mul_f32 %0, %2, %0\n\tv_accvgpr_write_b32 a62, %0\n\t" "v_accvgpr_read_b32 %1, a63\n\tv_mul_f32 %1, %2, %1\n\tv_accvgpr_write_b32 a63, %1\n\t" : "=&v"(t0), "=&v"(t1) : "v"(alv[0]) : ACLOB); }
      { float t0, t1; asm volatile("v_accvgpr_read_b32 %0, a64\n\tv_mul_f32 %0, %2, %0\n\tv_accvgpr_write_b32 a64, %0\n\t" "v_accvgpr_read_b32 %1, a65\n\tv_mul_f32 %1, %2, %1\n\tv_accvgpr_write_b32 a65, %1\n\t" "v_accvgpr_read_b32 %0, a66\n\tv_mul_f32 %0, %2, %0\n\tv_accvgpr_write_b32 a66, %0\n\t" "v_accvgpr_read_b32 %1, a67\n\tv_mul_f32 %1, %2, %1\n\tv_accvgpr_write_b32 a67, %1\n\t" "v_accvgpr_read_b32 %0, a68\n\tv_mul_f32 %0, %2, %0\n\tv_accvgpr_write_b32 a68, %0\n\t" "v_accvgpr_read_b32 %1, a69\n\tv_mul_f32 %1, %2, %1\n\tv_accvgpr_write_b32 a69, %1\n\t" "v_accvgpr_read_b32 %0, a70\n\tv_mul_f32 %0, %2, %0\n\tv_accvgpr_write_b32 a70, %0\n\t" "v_accvgpr_read_b32 %1, a71\n\tv_mul_f32 %1, %2, %1\n\tv_accvgpr_write_b32 a71, %1\n\t" "v_accvgpr_read_b32 %0, a72\n\tv_mul_f32 %0, %2, %0\n\tv_accvgpr_write_b32 a72, %0\n\t" "v_accvgpr_read_b32 %1, a73\n\tv_mul_f32 %1, %2, %1\n\tv_accvgpr_write_b32 a73, %1\n\t" "v_accvgpr_read_b32 %0, a74\n\tv_mul_f32 %0, %2, %0\n\tv_accvgpr_write_b32 a74, %0\n\t" "v_accvgpr_read_b32 %1, a75\n\tv_mul_f32 %1, %2, %1\n\tv_accvgpr_write_b32 a75, %1\n\t" "v_accvgpr_read_b32 %0, a76\n\tv_mul_f32 %0, %2, %0\n\tv_accvgpr_write_b32 a76, %0\n\t" "v_accvgpr_read_b32 %1, a77\n\tv_mul_f32 %1, %2, %1\n\tv_accvgpr_write_b32 a77, %1\n\t" "v_accvgpr_read_b32 %0, a78\n\tv_mul_f32 %0, %2, %0\n\tv_accvgpr_write_b32 a78, %0\n\t" "v_accvgpr_read_b32 %1, a79\n\tv_mul_f32 %1, %2, %1\n\tv_accvgpr_write_b32 a79, %1\n\t" "v_accvgpr_read_b32 %0, a80\n\tv_mul_f32 %0, %2, %0\n\tv_accvgpr_write_b32 a80, %0\n\t" "v_accvgpr_read_b32 %1, a81\n\tv_mul_f32 %1, %2, %1\n\tv_accvgpr_write_b32 a81, %1\n\t" "v_accvgpr_read_b32 %0, a82\n\tv_mul_f32 %0, %2, %0\n\tv_accvgpr_write_b32 a82, %0\n\t" "v_accvgpr_read_b32 %1, a83\n\tv_mul_f32 %1, %2, %1\n\tv_accvgpr_write_b32 a83, %1\n\t" "v_accvgpr_read_b32 %0, a84\n\tv_mul_f32 %0, %2, %0\n\tv_accvgpr_write_b32 a84, %0\n\t" "v_accvgpr_read_b32 %1, a85\n\tv_mul_f32 %1, %2, %1\n\tv_accvgpr_write_b32 a85, %1\n\t" "v_accvgpr_read_b32 %0, a86\n\tv_mul_f32 %0, %2, %0\n\tv_accvgpr_write_b32 a86, %0\n\t" "v_accvgpr_read_b32 %1, a87\n\tv_mul_f32 %1, %2, %1\n\tv_accvgpr_write_b32 a87, %1\n\t" "v_accvgpr_read_b32 %0, a88\n\tv_mul_f32 %0, %2, %0\n\tv_accvgpr_write_b32 a88, %0\n\t" "v_accvgpr_read_b32 %1, a89\n\tv_mul_f32 %1, %2, %1\n\tv_accvgpr_write_b32 a89, %1\n\t" "v_accvgpr_read_b32 %0, a90\n\tv_mul_f32 %0, %2, %0\n\tv_accvgpr_write_b32 a90, %0\n\t" "v_accvgpr_read_b32 %1, a91\n\tv_mul_f32 %1, %2, %1\n\tv_accvgpr_write_b32 a91, %1\n\t" "v_accvgpr_read_b32 %0, a92\n\tv_mul_f32 %0, %2, %0\n\tv_accvgpr_write_b32 a92, %0\n\t" "v_accvgpr_read_b32 %1, a93\n\tv_mul_f32 %1, %2, %1\n\tv_accvgpr_write_b32 a93, %1\n\t" "v_accvgpr_read_b32 %0, a94\n\tv_mul_f32 %0, %2, %0\n\tv_accvgpr_write_b32 a94, %0\n\t" "v_accvgpr_read_b32 %1, a95\n\tv_mul_f32 %1, %2, %1\n\tv_accvgpr_write_b32 a95, %1\n\t" "v_accvgpr_read_b32 %0, a96\n\tv_mul_f32 %0, %2, %0\n\tv_accvgpr_write_b32 a96, %0\n\t" "v_accvgpr_read_b32 %1, a97\n\tv_mul_f32 %1, %2, %1\n\tv_accvgpr_write_b32 a97, %1\n\t" "v_accvgpr_read_b32 %0, a98\n\tv_mul_f32 %0, %2, %0\n\tv_accvgpr_write_b32 a98, %0\n\t" "v_accvgpr_read_b32 %1, a99\n\tv_mul_f32 %1, %2, %1\n\tv_accvgpr_write_b32 a99, %1\n\t" "v_accvgpr_read_b32 %0, a100\n\tv_mul_f32 %0, %2, %0\n\tv_accvgpr_write_b32 a100, %0\n\t" "v_accvgpr_read_b32 %1, a101\n\tv_mul_f32 %1, %2, %1\n\tv_accvgpr_write_b32 a101, %1\n\t" "v_accvgpr_read_b32 %0, a102\n\tv_mul_f32 %0, %2, %0\n\tv_accvgpr_write_b32 a102, %0\n\t" "v_accvgpr_read_b32 %1, a103\n\tv_mul_f32 %1, %2, %1\n\tv_accvgpr_write_b32 a103, %1\n\t" "v_accvgpr_read_b32 %0, a104\n\tv_mul_f32 %0, %2, %0\n\tv_accvgpr_write_b32 a104, %0\n\t" "v_accvgpr_read_b32 %1, a105\n\tv_mul_f32 %1, %2, %1\n\tv_accvgpr_write_b32 a105, %1\n\t" "v_accvgpr_read_b32 %0, a106\n\tv_mul_f32 %0, %2, %0\n\tv_accvgpr_write_b32 a106, %0\n\t" "v_accvgpr_read_b32 %1, a107\n\tv_mul_f32 %1, %2, %1\n\tv_accvgpr_write_b32 a107, %1\n\t" "v_accvgpr_read_b32 %0, a108\n\tv_mul_f32 %0, %2, %0\n\tv_accvgpr_write_b32 a108, %0\n\t" "v_accvgpr_read_b32 %1, a109\n\tv_mul_f32 %1, %2, %1\n\tv_accvgpr_write_b32 a109, %1\n\t" "v_accvgpr_read_b32 %0, a110\n\tv_mul_f32 %0, %2, %0\n\tv_accvgpr_write_b32 a110, %0\n\t" "v_accvgpr_read_b32 %1, a111\n\tv_mul_f32 %1, %2, %1\n\tv_accvgpr_write_b32 a111, %1\n\t" "v_accvgpr_read_b32 %0, a112\n\tv_mul_f32 %0, %2, %0\n\tv_accvgpr_write_b32 a112, %0\n\t" "v_accvgpr_read_b32 %1, a113\n\tv_mul_f32 %1, %2, %1\n\tv_accvgpr_write_b32 a113, %1\n\t" "v_accvgpr_read_b32 %0, a114\n\tv_mul_f32 %0, %2, %0\n\tv_accvgpr_write_b32 a114, %0\n\t" "v_accvgpr_read_b32 %1, a115\n\tv_mul_f32 %1, %2, %1\n\tv_accvgpr_write_b32 a115, %1\n\t" "v_accvgpr_read_b32 %0, a116\n\tv_mul_f32 %0, %2, %0\n\tv_accvgpr_write_b32 a116, %0\n\t" "v_accvgpr_read_b32 %1, a117\n\tv_mul_f32 %1, %2, %1\n\tv_accvgpr_write_b32 a117, %1\n\t" "v_accvgpr_read_b32 %0, a118\n\tv_mul_f32 %0, %2, %0\n\tv_accvgpr_write_b32 a118, %0\n\t" "v_accvgpr_read_b32 %1, a119\n\tv_mul_f32 %1, %2, %1\n\tv_accvgpr_write_b32 a119, %1\n\t" "v_accvgpr_read_b32 %0, a120\n\tv_mul_f32 %0, %2, %0\n\tv_accvgpr_write_b32 a120, %0\n\t" "v_accvgpr_read_b32 %1, a121\n\tv_mul_f32 %1, %2, %1\n\tv_accvgpr_write_b32 a121, %1\n\t" "v_accvgpr_read_b32 %0, a122\n\tv_mul_f32 %0, %2, %0\n\tv_accvgpr_write_b32 a122, %0\n\t" "v_accvgpr_read_b32 %1, a123\n\tv_mul_f32 %1, %2, %1\n\tv_accvgpr_write_b32 a123, %1\n\t" "v_accvgpr_read_b32 %0, a124\n\tv_mul_f32 %0, %2, %0\n\tv_accvgpr_write_b32 a124, %0\n\t" "v_accvgpr_read_b32 %1, a125\n\tv_mul_f32 %1, %2, %1\n\tv_accvgpr_write_b32 a125, %1\n\t" "v_accvgpr_read_b32 %0, a126\n\tv_mul_f32 %0, %2, %0\n\tv_accvgpr_write_b32 a126, %0\n\t" "v_accvgpr_read_b32 %1, a127\n\tv_mul_f32 %1, %2, %1\n\tv_accvgpr_write_b32 a127, %1\n\t" : "=&v"(t0), "=&v"(t1) : "v"(alv[1]) : ACLOB); }
      { float t0, t1; asm volatile("v_accvgpr_read_b32 %0, a128\n\tv_mul_f32 %0, %2, %0\n\tv_accvgpr_write_b32 a128, %0\n\t" "v_accvgpr_read_b32 %1, a129\n\tv_mul_f32 %1, %2, %1\n\tv_accvgpr_write_b32 a129, %1\n\t" "v_accvgpr_read_b32 %0, a130\n\tv_mul_f32 %0, %2, %0\n\tv_accvgpr_write_b32 a130, %0\n\t" "v_accvgpr_read_b32 %1, a131\n\tv_mul_f32 %1, %2, %1\n\tv_accvgpr_write_b32 a131, %1\n\t" "v_accvgpr_read_b32 %0, a132\n\tv_mul_f32 %0, %2, %0\n\tv_accvgpr_write_b32 a132, %0\n\t" "v_accvgpr_read_b32 %1, a133\n\tv_mul_f32 %1, %2, %1\n\tv_accvgpr_write_b32 a133, %1\n\t" "v_accvgpr_read_b32 %0, a134\n\tv_mul_f32 %0, %2, %0\n\tv_accvgpr_write_b32 a134, %0\n\t" "v_accvgpr_read_b32 %1, a135\n\tv_mul_f32 %1, %2, %1\n\tv_accvgpr_write_b32 a135, %1\n\t" "v_accvgpr_read_b32 %0, a136\n\tv_mul_f32 %0, %2, %0\n\tv_accvgpr_write_b32 a136, %0\n\t" "v_accvgpr_read_b32 %1, a137\n\tv_mul_f32 %1, %2, %1\n\tv_accvgpr_write_b32 a137, %1\n\t" "v_accvgpr_read_b32 %0, a138\n\tv_mul_f32 %0, %2, %0\n\tv_accvgpr_write_b32 a138, %0\n\t" "v_accvgpr_read_b32 %1, a139\n\tv_mul_f32 %1, %2, %1\n\tv_accvgpr_write_b32 a139, %1\n\t" "v_accvgpr_read_b32 %0, a140\n\tv_mul_f32 %0, %2, %0\n\tv_accvgpr_write_b32 a140, %0\n\t" "v_accvgpr_read_b32 %1, a141\n\tv_mul_f32 %1, %2, %1\n\tv_accvgpr_write_b32 a141, %1\n\t" "v_accvgpr_read_b32 %0, a142\n\tv_mul_f32 %0, %2, %0\n\tv_accvgpr_write_b32 a142, %0\n\t" "v_accvgpr_read_b32 %1, a143\n\tv_mul_f32 %1, %2, %1\n\tv_accvgpr_write_b32 a143, %1\n\t" "v_accvgpr_read_b32 %0, a144\n\tv_mul_f32 %0, %2, %0\n\tv_accvgpr_write_b32 a144, %0\n\t" "v_accvgpr_read_b32 %1, a145\n\tv_mul_f32 %1, %2, %1\n\tv_accvgpr_write_b32 a145, %1\n\t" "v_accvgpr_read_b32 %0, a146\n\tv_mul_f32 %0, %2, %0\n\tv_accvgpr_write_b32 a146, %0\n\t" "v_accvgpr_read_b32 %1, a147\n\tv_mul_f32 %1, %2, %1\n\tv_accvgpr_write_b32 a147, %1\n\t" "v_accvgpr_read_b32 %0, a148\n\tv_mul_f32 %0, %2, %0\n\tv_accvgpr_write_b32 a148, %0\n\t" "v_accvgpr_read_b32 %1, a149\n\tv_mul_f32 %1, %2, %1\n\tv_accvgpr_write_b32 a149, %1\n\t" "v_accvgpr_read_b32 %0, a150\n\tv_mul_f32 %0, %2, %0\n\tv_accvgpr_write_b32 a150, %0\n\t" "v_accvgpr_read_b32 %1, a151\n\tv_mul_f32 %1, %2, %1\n\tv_accvgpr_write_b32 a151, %1\n\t" "v_accvgpr_read_b32 %0, a152\n\tv_mul_f32 %0, %2, %0\n\tv_accvgpr_write_b32 a152, %0\n\t" "v_accvgpr_read_b32 %1, a153\n\tv_mul_f32 %1, %2, %1\n\tv_accvgpr_write_b32 a153, %1\n\t" "v_accvgpr_read_b32 %0, a154\n\tv_mul_f32 %0, %2, %0\n\tv_accvgpr_write_b32 a154, %0\n\t" "v_accvgpr_read_b32 %1, a155\n\tv_mul_f32 %1, %2, %1\n\tv_accvgpr_write_b32 a155, %1\n\t" "v_accvgpr_read_b32 %0, a156\n\tv_mul_f32 %0, %2, %0\n\tv_accvgpr_write_b32 a156, %0\n\t" "v_accvgpr_read_b32 %1, a157\n\tv_mul_f32 %1, %2, %1\n\tv_accvgpr_write_b32 a157, %1\n\t" "v_accvgpr_read_b32 %0, a158\n\tv_mul_f32 %0, %2, %0\n\tv_accvgpr_write_b32 a158, %0\n\t" "v_accvgpr_read_b32 %1, a159\n\tv_mul_f32 %1, %2, %1\n\tv_accvgpr_write_b32 a159, %1\n\t" "v_accvgpr_read_b32 %0, a160\n\tv_mul_f32 %0, %2, %0\n\tv_accvgpr_write_b32 a160, %0\n\t" "v_accvgpr_read_b32 %1, a161\n\tv_mul_f32 %1, %2, %1\n\tv_accvgpr_write_b32 a161, %1\n\t" "v_accvgpr_read_b32 %0, a162\n\tv_mul_f32 %0, %2, %0\n\tv_accvgpr_write_b32 a162, %0\n\t" "v_accvgpr_read_b32 %1, a163\n\tv_mul_f32 %1, %2, %1\n\tv_accvgpr_write_b32 a163, %1\n\t" "v_accvgpr_read_b32 %0, a164\n\tv_mul_f32 %0, %2, %0\n\tv_accvgpr_write_b32 a164, %0\n\t" "v_accvgpr_read_b32 %1, a165\n\tv_mul_f32 %1, %2, %1\n\tv_accvgpr_write_b32 a165, %1\n\t" "v_accvgpr_read_b32 %0, a166\n\tv_mul_f32 %0, %2, %0\n\tv_accvgpr_write_b32 a166, %0\n\t" "v_accvgpr_read_b32 %1, a167\n\tv_mul_f32 %1, %2, %1\n\tv_accvgpr_write_b32 a167, %1\n\t" "v_accvgpr_read_b32 %0, a168\n\tv_mul_f32 %0, %2, %0\n\tv_accvgpr_write_b32 a168, %0\n\t" "v_accvgpr_read_b32 %1, a169\n\tv_mul_f32 %1, %2, %1\n\tv_accvgpr_write_b32 a169, %1\n\t" "v_accvgpr_read_b32 %0, a170\n\tv_mul_f32 %0, %2, %0\n\tv_accvgpr_write_b32 a170, %0\n\t" "v_accvgpr_read_b32 %1, a171\n\tv_mul_f32 %1, %2, %1\n\tv_accvgpr_write_b32 a171, %1\n\t" "v_accvgpr_read_b32 %0, a172\n\tv_mul_f32 %0, %2, %0\n\tv_accvgpr_write_b32 a172, %0\n\t" "v_accvgpr_read_b32 %1, a173\n\tv_mul_f32 %1, %2, %1\n\tv_accvgpr_write_b32 a173, %1\n\t" "v_accvgpr_read_b32 %0, a174\n\tv_mul_f32 %0, %2, %0\n\tv_accvgpr_write_b32 a174, %0\n\t" "v_accvgpr_read_b32 %1, a175\n\tv_mul_f32 %1, %2, %1\n\tv_accvgpr_write_b32 a175, %1\n\t" "v_accvgpr_read_b32 %0, a176\n\tv_mul_f32 %0, %2, %0\n\tv_accvgpr_write_b32 a176, %0\n\t" "v_accvgpr_read_b32 %1, a177\n\tv_mul_f32 %1, %2, %1\n\tv_accvgpr_write_b32 a177, %1\n\t" "v_accvgpr_read_b32 %0, a178\n\tv_mul_f32 %0, %2, %0\n\tv_accvgpr_write_b32 a178, %0\n\t" "v_accvgpr_read_b32 %1, a179\n\tv_mul_f32 %1, %2, %1\n\tv_accvgpr_write_b32 a179, %1\n\t" "v_accvgpr_read_b32 %0, a180\n\tv_mul_f32 %0, %2, %0\n\tv_accvgpr_write_b32 a180, %0\n\t" "v_accvgpr_read_b32 %1, a181\n\tv_mul_f32 %1, %2, %1\n\tv_accvgpr_write_b32 a181, %1\n\t" "v_accvgpr_read_b32 %0, a182\n\tv_mul_f32 %0, %2, %0\n\tv_accvgpr_write_b32 a182, %0\n\t" "v_accvgpr_read_b32 %1, a183\n\tv_mul_f32 %1, %2, %1\n\tv_accvgpr_write_b32 a183, %1\n\t" "v_accvgpr_read_b32 %0, a184\n\tv_mul_f32 %0, %2, %0\n\tv_accvgpr_write_b32 a184, %0\n\t" "v_accvgpr_read_b32 %1, a185\n\tv_mul_f32 %1, %2, %1\n\tv_accvgpr_write_b32 a185, %1\n\t" "v_accvgpr_read_b32 %0, a186\n\tv_mul_f32 %0, %2, %0\n\tv_accvgpr_write_b32 a186, %0\n\t" "v_accvgpr_read_b32 %1, a187\n\tv_mul_f32 %1, %2, %1\n\tv_accvgpr_write_b32 a187, %1\n\t" "v_accvgpr_read_b32 %0, a188\n\tv_mul_f32 %0, %2, %0\n\tv_accvgpr_write_b32 a188, %0\n\t" "v_accvgpr_read_b32 %1, a189\n\tv_mul_f32 %1, %2, %1\n\tv_accvgpr_write_b32 a189, %1\n\t" "v_accvgpr_read_b32 %0, a190\n\tv_mul_f32 %0, %2, %0\n\tv_accvgpr_write_b32 a190, %0\n\t" "v_accvgpr_read_b32 %1, a191\n\tv_mul_f32 %1, %2, %1\n\tv_accvgpr_write_b32 a191, %1\n\t" : "=&v"(t0), "=&v"(t1) : "v"(alv[2]) : ACLOB); }
    asm volatile("s_nop 1" ::: "memory"); pend = 0;
  }
  SBAR();
  asm volatile("s_waitcnt vmcnt(0)\n\ts_barrier" ::: "memory");
  vf[0] = VRD(1, 0, 0); vf[1] = VRD(1, 0, 1); vf[2] = VRD(1, 0, 2); vf[3] = VRD(1, 0, 3); vf[4] = VRD(1, 0, 4); vf[5] = VRD(1, 0, 5);
  sB[2][2][0] = exp_s(ffma_s(sB[2][2][0], csc, nmc[2])); PIN(sB[2][2][0]);
  sB[2][2][1] = exp_s(ffma_s(sB[2][2][1], csc, nmc[2])); PIN(sB[2][2][1]);
  sB[2][2][2] = exp_s(ffma_s(sB[2][2][2], csc, nmc[2])); PIN(sB[2][2][2]);
  sB[2][2][3] = exp_s(ffma_s(sB[2][2][3], csc, nmc[2])); PIN(sB[2][2][3]);
  sB[2][3][0] = exp_s(ffma_s(sB[2][3][0], csc, nmc[2])); PIN(sB[2][3][0]);
  sB[2][3][1] = exp_s(ffma_s(sB[2][3][1], csc, nmc[2])); PIN(sB[2][3][1]);
  sB[2][3][2] = exp_s(ffma_s(sB[2][3][2], csc, nmc[2])); PIN(sB[2][3][2]);
  sB[2][3][3] = exp_s(ffma_s(sB[2][3][3], csc, nmc[2])); PIN(sB[2][3][3]);
  l[0] = fadd_s(l[0], sB[0][0][0]); PIN(l[0]);
  l[0] = fadd_s(l[0], sB[0][0][1]); PIN(l[0]); pw[0][0][0] = cvtpk(sB[0][0][0], sB[0][0][1]); PIN(pw[0][0][0]);
  l[0] = fadd_s(l[0], sB[0][0][2]); PIN(l[0]);
  l[0] = fadd_s(l[0], sB[0][0][3]); PIN(l[0]); pw[0][0][1] = cvtpk(sB[0][0][2], sB[0][0][3]); PIN(pw[0][0][1]);
  l[0] = fadd_s(l[0], sB[0][1][0]); PIN(l[0]);
  l[0] = fadd_s(l[0], sB[0][1][1]); PIN(l[0]); pw[0][0][2] = cvtpk(sB[0][1][0], sB[0][1][1]); PIN(pw[0][0][2]);
  l[0] = fadd_s(l[0], sB[0][1][2]); PIN(l[0]);
  l[0] = fadd_s(l[0], sB[0][1][3]); PIN(l[0]); pw[0][0][3] = cvtpk(sB[0][1][2], sB[0][1][3]); PIN(pw[0][0][3]);
  l[0] = fadd_s(l[0], sB[0][2][0]); PIN(l[0]);
  l[0] = fadd_s(l[0], sB[0][2][1]); PIN(l[0]); pw[0][1][0] = cvtpk(sB[0][2][0], sB[0][2][1]); PIN(pw[0][1][0]);
  l[0] = fadd_s(l[0], sB[0][2][2]); PIN(l[0]);
  l[0] = fadd_s(l[0], sB[0][2][3]); PIN(l[0]); pw[0][1][1] = cvtpk(sB[0][2][2], sB[0][2][3]); PIN(pw[0][1][1]);
  l[0] = fadd_s(l[0], sB[0][3][0]); PIN(l[0]);
  l[0] = fadd_s(l[0], sB[0][3][1]); PIN(l[0]); pw[0][1][2] = cvtpk(sB[0][3][0], sB[0][3][1]); PIN(pw[0][1][2]);
  l[0] = fadd_s(l[0], sB[0][3][2]); PIN(l[0]);
  l[0] = fadd_s(l[0], sB[0][3][3]); PIN(l[0]); pw[0][1][3] = cvtpk(sB[0][3][2], sB[0][3][3]); PIN(pw[0][1][3]);
  l[1] = fadd_s(l[1], sB[1][0][0]); PIN(l[1]);
  l[1] = fadd_s(l[1], sB[1][0][1]); PIN(l[1]); pw[1][0][0] = cvtpk(sB[1][0][0], sB[1][0][1]); PIN(pw[1][0][0]);
  l[1] = fadd_s(l[1], sB[1][0][2]); PIN(l[1]);
  l[1] = fadd_s(l[1], sB[1][0][3]); PIN(l[1]); pw[1][0][1] = cvtpk(sB[1][0][2], sB[1][0][3]); PIN(pw[1][0][1]);
  l[1] = fadd_s(l[1], sB[1][1][0]); PIN(l[1]);
  l[1] = fadd_s(l[1], sB[1][1][1]); PIN(l[1]); pw[1][0][2] = cvtpk(sB[1][1][0], sB[1][1][1]); PIN(pw[1][0][2]);
  l[1] = fadd_s(l[1], sB[1][1][2]); PIN(l[1]);
  l[1] = fadd_s(l[1], sB[1][1][3]); PIN(l[1]); pw[1][0][3] = cvtpk(sB[1][1][2], sB[1][1][3]); PIN(pw[1][0][3]);
  l[1] = fadd_s(l[1], sB[1][2][0]); PIN(l[1]);
  l[1] = fadd_s(l[1], sB[1][2][1]); PIN(l[1]); pw[1][1][0] = cvtpk(sB[1][2][0], sB[1][2][1]); PIN(pw[1][1][0]);
  l[1] = fadd_s(l[1], sB[1][2][2]); PIN(l[1]);
  l[1] = fadd_s(l[1], sB[1][2][3]); PIN(l[1]); pw[1][1][1] = cvtpk(sB[1][2][2], sB[1][2][3]); PIN(pw[1][1][1]);
  l[1] = fadd_s(l[1], sB[1][3][0]); PIN(l[1]);
  l[1] = fadd_s(l[1], sB[1][3][1]); PIN(l[1]); pw[1][1][2] = cvtpk(sB[1][3][0], sB[1][3][1]); PIN(pw[1][1][2]);
  l[1] = fadd_s(l[1], sB[1][3][2]); PIN(l[1]);
  l[1] = fadd_s(l[1], sB[1][3][3]); PIN(l[1]); pw[1][1][3] = cvtpk(sB[1][3][2], sB[1][3][3]); PIN(pw[1][1][3]);
  l[2] = fadd_s(l[2], sB[2][0][0]); PIN(l[2]);
  l[2] = fadd_s(l[2], sB[2][0][1]); PIN(l[2]); pw[2][0][0] = cvtpk(sB[2][0][0], sB[2][0][1]); PIN(pw[2][0][0]);
  l[2] = fadd_s(l[2], sB[2][0][2]); PIN(l[2]);
  l[2] = fadd_s(l[2], sB[2][0][3]); PIN(l[2]); pw[2][0][1] = cvtpk(sB[2][0][2], sB[2][0][3]); PIN(pw[2][0][1]);
  l[2] = fadd_s(l[2], sB[2][1][0]); PIN(l[2]);
  l[2] = fadd_s(l[2], sB[2][1][1]); PIN(l[2]); pw[2][0][2] = cvtpk(sB[2][1][0], sB[2][1][1]); PIN(pw[2][0][2]);
  l[2] = fadd_s(l[2], sB[2][1][2]); PIN(l[2]);
  l[2] = fadd_s(l[2], sB[2][1][3]); PIN(l[2]); pw[2][0][3] = cvtpk(sB[2][1][2], sB[2][1][3]); PIN(pw[2][0][3]);
  l[2] = fadd_s(l[2], sB[2][2][0]); PIN(l[2]);
  l[2] = fadd_s(l[2], sB[2][2][1]); PIN(l[2]); pw[2][1][0] = cvtpk(sB[2][2][0], sB[2][2][1]); PIN(pw[2][1][0]);
  l[2] = fadd_s(l[2], sB[2][2][2]); PIN(l[2]);
  l[2] = fadd_s(l[2], sB[2][2][3]); PIN(l[2]); pw[2][1][1] = cvtpk(sB[2][2][2], sB[2][2][3]); PIN(pw[2][1][1]);
  l[2] = fadd_s(l[2], sB[2][3][0]); PIN(l[2]);
  l[2] = fadd_s(l[2], sB[2][3][1]); PIN(l[2]); pw[2][1][2] = cvtpk(sB[2][3][0], sB[2][3][1]); PIN(pw[2][1][2]);
  l[2] = fadd_s(l[2], sB[2][3][2]); PIN(l[2]);
  l[2] = fadd_s(l[2], sB[2][3][3]); PIN(l[2]); pw[2][1][3] = cvtpk(sB[2][3][2], sB[2][3][3]); PIN(pw[2][1][3]);
  _Pragma("unroll") for (int sb = 0; sb < 3; ++sb) { pf[sb][0] = __builtin_bit_cast(bf16x8, pw[sb][0]); pf[sb][1] = __builtin_bit_cast(bf16x8, pw[sb][1]); PIN(pf[sb][0]); PIN(pf[sb][1]); }
  asm volatile("s_nop 1" ::: "memory"); SBAR();
  { MF_O(0, vf[0], pf[0][0]); vf[6] = VRD(1, 0, 6); SBAR(); }
  { MF_O(64, vf[0], pf[1][0]);  SBAR(); }
  { MF_O(128, vf[0], pf[2][0]);  SBAR(); }
  { MF_O(4, vf[1], pf[0][0]); vf[7] = VRD(1, 0, 7); SBAR(); }
  { MF_O(68, vf[1], pf[1][0]);  SBAR(); }
  { MF_O(132, vf[1], pf[2][0]);  SBAR(); }
  { MF_O(8, vf[2], pf[0][0]); vf[0] = VRD(1, 0, 8); SBAR(); }
  { MF_O(72, vf[2], pf[1][0]);  SBAR(); }
  { MF_O(136, vf[2], pf[2][0]);  SBAR(); }
  { MF_O(12, vf[3], pf[0][0]); vf[1] = VRD(1, 0, 9); SBAR(); }
  { MF_O(76, vf[3], pf[1][0]);  SBAR(); }
  { MF_O(140, vf[3], pf[2][0]);  SBAR(); }
  { MF_O(16, vf[4], pf[0][0]); vf[2] = VRD(1, 0, 10); SBAR(); }
  { MF_O(80, vf[4], pf[1][0]);  SBAR(); }
  { MF_O(144, vf[4], pf[2][0]);  SBAR(); }
  { MF_O(20, vf[5], pf[0][0]); vf[3] = VRD(1, 0, 11); SBAR(); }
  { MF_O(84, vf[5], pf[1][0]);  SBAR(); }
  { MF_O(148, vf[5], pf[2][0]);  SBAR(); }
  { MF_O(24, vf[6], pf[0][0]); vf[4] = VRD(1, 0, 12); SBAR(); }
  { MF_O(88, vf[6], pf[1][0]);  SBAR(); }
  { MF_O(152, vf[6], pf[2][0]);  SBAR(); }
  { MF_O(28, vf[7], pf[0][0]); vf[5] = VRD(1, 0, 13); SBAR(); }
  { MF_O(92, vf[7], pf[1][0]);  SBAR(); }
  { MF_O(156, vf[7], pf[2][0]);  SBAR(); }
  { MF_O(32, vf[0], pf[0][0]); vf[6] = VRD(1, 0, 14); SBAR(); }
  { MF_O(96, vf[0], pf[1][0]);  SBAR(); }
  { MF_O(160, vf[0], pf[2][0]);  SBAR(); }
  { MF_O(36, vf[1], pf[0][0]); vf[7] = VRD(1, 0, 15); SBAR(); }
  { MF_O(100, vf[1], pf[1][0]);  SBAR(); }
  { MF_O(164, vf[1], pf[2][0]);  SBAR(); }
  { MF_O(40, vf[2], pf[0][0]); vf[0] = VRD(1, 1, 0); SBAR(); }
  { MF_O(104, vf[2], pf[1][0]);  SBAR(); }
  { MF_O(168, vf[2], pf[2][0]);  SBAR(); }
  { MF_O(44, vf[3], pf[0][0]); vf[1] = VRD(1, 1, 1); SBAR(); }
  { MF_O(108, vf[3], pf[1][0]);  SBAR(); }
  { MF_O(172, vf[3], pf[2][0]);  SBAR(); }
  { MF_O(48, vf[4], pf[0][0]); vf[2] = VRD(1, 1, 2); SBAR(); }
  { MF_O(112, vf[4], pf[1][0]);  SBAR(); }
  { MF_O(176, vf[4], pf[2][0]);  SBAR(); }
  { MF_O(52, vf[5], pf[0][0]); vf[3] = VRD(1, 1, 3); SBAR(); }
  { MF_O(116, vf[5], pf[1][0]);  SBAR(); }
  { MF_O(180, vf[5], pf[2][0]);  SBAR(); }
  { MF_O(56, vf[6], pf[0][0]); vf[4] = VRD(1, 1, 4); SBAR(); }
  { MF_O(120, vf[6], pf[1][0]);  SBAR(); }
  { MF_O(184, vf[6], pf[2][0]);  SBAR(); }
  { MF_O(60, vf[7], pf[0][0]); vf[5] = VRD(1, 1, 5); SBAR(); }
  { MF_O(124, vf[7], pf[1][0]);  SBAR(); }
  { MF_O(188, vf[7], pf[2][0]);  SBAR(); }
  { MF_O(0, vf[0], pf[0][1]); vf[6] = VRD(1, 1, 6); SBAR(); }
  { MF_O(64, vf[0], pf[1][1]);  SBAR(); }
  { MF_O(128, vf[0], pf[2][1]);  SBAR(); }
  { MF_O(4, vf[1], pf[0][1]); vf[7] = VRD(1, 1, 7); SBAR(); }
  { MF_O(68, vf[1], pf[1][1]);  SBAR(); }
  { MF_O(132, vf[1], pf[2][1]);  SBAR(); }
  { MF_O(8, vf[2], pf[0][1]); vf[0] = VRD(1, 1, 8); SBAR(); }
  { MF_O(72, vf[2], pf[1][1]);  SBAR(); }
  { MF_O(136, vf[2], pf[2][1]);  SBAR(); }
  { MF_O(12, vf[3], pf[0][1]); vf[1] = VRD(1, 1, 9); SBAR(); }
  { MF_O(76, vf[3], pf[1][1]);  SBAR(); }
  { MF_O(140, vf[3], pf[2][1]);  SBAR(); }
  { MF_O(16, vf[4], pf[0][1]); vf[2] = VRD(1, 1, 10); SBAR(); }
  { MF_O(80, vf[4], pf[1][1]);  SBAR(); }
  { MF_O(144, vf[4], pf[2][1]);  SBAR(); }
  { MF_O(20, vf[5], pf[0][1]); vf[3] = VRD(1, 1, 11); SBAR(); }
  { MF_O(84, vf[5], pf[1][1]);  SBAR(); }
  { MF_O(148, vf[5], pf[2][1]);  SBAR(); }
  { MF_O(24, vf[6], pf[0][1]); vf[4] = VRD(1, 1, 12); SBAR(); }
  { MF_O(88, vf[6], pf[1][1]);  SBAR(); }
  { MF_O(152, vf[6], pf[2][1]);  SBAR(); }
  { MF_O(28, vf[7], pf[0][1]); vf[5] = VRD(1, 1, 13); SBAR(); }
  { MF_O(92, vf[7], pf[1][1]);  SBAR(); }
  { MF_O(156, vf[7], pf[2][1]);  SBAR(); }
  { MF_O(32, vf[0], pf[0][1]); vf[6] = VRD(1, 1, 14); SBAR(); }
  { MF_O(96, vf[0], pf[1][1]);  SBAR(); }
  { MF_O(160, vf[0], pf[2][1]);  SBAR(); }
  { MF_O(36, vf[1], pf[0][1]); vf[7] = VRD(1, 1, 15); SBAR(); }
  { MF_O(100, vf[1], pf[1][1]);  SBAR(); }
  { MF_O(164, vf[1], pf[2][1]);  SBAR(); }
  { MF_O(40, vf[2], pf[0][1]);  SBAR(); }
  { MF_O(104, vf[2], pf[1][1]);  SBAR(); }
  { MF_O(168, vf[2], pf[2][1]);  SBAR(); }
  { MF_O(44, vf[3], pf[0][1]);  SBAR(); }
  { MF_O(108, vf[3], pf[1][1]);  SBAR(); }
  { MF_O(172, vf[3], pf[2][1]);  SBAR(); }
  { MF_O(48, vf[4], pf[0][1]);  SBAR(); }
  { MF_O(112, vf[4], pf[1][1]);  SBAR(); }
  { MF_O(176, vf[4], pf[2][1]);  SBAR(); }
  { MF_O(52, vf[5], pf[0][1]);  SBAR(); }
  { MF_O(116, vf[5], pf[1][1]);  SBAR(); }
  { MF_O(180, vf[5], pf[2][1]);  SBAR(); }
  { MF_O(56, vf[6], pf[0][1]);  SBAR(); }
  { MF_O(120, vf[6], pf[1][1]);  SBAR(); }
  { MF_O(184, vf[6], pf[2][1]);  SBAR(); }
  { MF_O(60, vf[7], pf[0][1]);  SBAR(); }
  { MF_O(124, vf[7], pf[1][1]);  SBAR(); }
  { MF_O(188, vf[7], pf[2][1]);  SBAR(); }
  asm volatile("s_nop 7\n\ts_nop 7" ::: "memory");
  asm volatile("s_barrier" ::: "memory");
  { __attribute__((address_space(3))) char* ost = (__attribute__((address_space(3))) char*)smem + w * 24576;
  { const float inv = __builtin_amdgcn_rcpf(xadd32(xadd16(l[0])));
    { float x0, x1, x2, x3; ARD(x0, 0); ARD(x1, 1); ARD(x2, 2); ARD(x3, 3); u32x2 pk = {cvtpk(x0 * inv, x1 * inv), cvtpk(x2 * inv, x3 * inv)};
      *(__attribute__((address_space(3))) u32x2*)(ost + (0 + c) * 512 + (((0 + (g >> 1)) ^ (c & 15)) << 4) + (g & 1) * 8) = pk; }
    { float x0, x1, x2, x3; ARD(x0, 4); ARD(x1, 5); ARD(x2, 6); ARD(x3, 7); u32x2 pk = {cvtpk(x0 * inv, x1 * inv), cvtpk(x2 * inv, x3 * inv)};
      *(__attribute__((address_space(3))) u32x2*)(ost + (0 + c) * 512 + (((2 + (g >> 1)) ^ (c & 15)) << 4) + (g & 1) * 8) = pk; }
    { float x0, x1, x2, x3; ARD(x0, 8); ARD(x1, 9); ARD(x2, 10); ARD(x3, 11); u32x2 pk = {cvtpk(x0 * inv, x1 * inv), cvtpk(x2 * inv, x3 * inv)};
      *(__attribute__((address_space(3))) u32x2*)(ost + (0 + c) * 512 + (((4 + (g >> 1)) ^ (c & 15)) << 4) + (g & 1) * 8) = pk; }
    { float x0, x1, x2, x3; ARD(x0, 12); ARD(x1, 13); ARD(x2, 14); ARD(x3, 15); u32x2 pk = {cvtpk(x0 * inv, x1 * inv), cvtpk(x2 * inv, x3 * inv)};
      *(__attribute__((address_space(3))) u32x2*)(ost + (0 + c) * 512 + (((6 + (g >> 1)) ^ (c & 15)) << 4) + (g & 1) * 8) = pk; }
    { float x0, x1, x2, x3; ARD(x0, 16); ARD(x1, 17); ARD(x2, 18); ARD(x3, 19); u32x2 pk = {cvtpk(x0 * inv, x1 * inv), cvtpk(x2 * inv, x3 * inv)};
      *(__attribute__((address_space(3))) u32x2*)(ost + (0 + c) * 512 + (((8 + (g >> 1)) ^ (c & 15)) << 4) + (g & 1) * 8) = pk; }
    { float x0, x1, x2, x3; ARD(x0, 20); ARD(x1, 21); ARD(x2, 22); ARD(x3, 23); u32x2 pk = {cvtpk(x0 * inv, x1 * inv), cvtpk(x2 * inv, x3 * inv)};
      *(__attribute__((address_space(3))) u32x2*)(ost + (0 + c) * 512 + (((10 + (g >> 1)) ^ (c & 15)) << 4) + (g & 1) * 8) = pk; }
    { float x0, x1, x2, x3; ARD(x0, 24); ARD(x1, 25); ARD(x2, 26); ARD(x3, 27); u32x2 pk = {cvtpk(x0 * inv, x1 * inv), cvtpk(x2 * inv, x3 * inv)};
      *(__attribute__((address_space(3))) u32x2*)(ost + (0 + c) * 512 + (((12 + (g >> 1)) ^ (c & 15)) << 4) + (g & 1) * 8) = pk; }
    { float x0, x1, x2, x3; ARD(x0, 28); ARD(x1, 29); ARD(x2, 30); ARD(x3, 31); u32x2 pk = {cvtpk(x0 * inv, x1 * inv), cvtpk(x2 * inv, x3 * inv)};
      *(__attribute__((address_space(3))) u32x2*)(ost + (0 + c) * 512 + (((14 + (g >> 1)) ^ (c & 15)) << 4) + (g & 1) * 8) = pk; }
    { float x0, x1, x2, x3; ARD(x0, 32); ARD(x1, 33); ARD(x2, 34); ARD(x3, 35); u32x2 pk = {cvtpk(x0 * inv, x1 * inv), cvtpk(x2 * inv, x3 * inv)};
      *(__attribute__((address_space(3))) u32x2*)(ost + (0 + c) * 512 + (((16 + (g >> 1)) ^ (c & 15)) << 4) + (g & 1) * 8) = pk; }
    { float x0, x1, x2, x3; ARD(x0, 36); ARD(x1, 37); ARD(x2, 38); ARD(x3, 39); u32x2 pk = {cvtpk(x0 * inv, x1 * inv), cvtpk(x2 * inv, x3 * inv)};
      *(__attribute__((address_space(3))) u32x2*)(ost + (0 + c) * 512 + (((18 + (g >> 1)) ^ (c & 15)) << 4) + (g & 1) * 8) = pk; }
    { float x0, x1, x2, x3; ARD(x0, 40); ARD(x1, 41); ARD(x2, 42); ARD(x3, 43); u32x2 pk = {cvtpk(x0 * inv, x1 * inv), cvtpk(x2 * inv, x3 * inv)};
      *(__attribute__((address_space(3))) u32x2*)(ost + (0 + c) * 512 + (((20 + (g >> 1)) ^ (c & 15)) << 4) + (g & 1) * 8) = pk; }
    { float x0, x1, x2, x3; ARD(x0, 44); ARD(x1, 45); ARD(x2, 46); ARD(x3, 47); u32x2 pk = {cvtpk(x0 * inv, x1 * inv), cvtpk(x2 * inv, x3 * inv)};
      *(__attribute__((address_space(3))) u32x2*)(ost + (0 + c) * 512 + (((22 + (g >> 1)) ^ (c & 15)) << 4) + (g & 1) * 8) = pk; }
    { float x0, x1, x2, x3; ARD(x0, 48); ARD(x1, 49); ARD(x2, 50); ARD(x3, 51); u32x2 pk = {cvtpk(x0 * inv, x1 * inv), cvtpk(x2 * inv, x3 * inv)};
      *(__attribute__((address_space(3))) u32x2*)(ost + (0 + c) * 512 + (((24 + (g >> 1)) ^ (c & 15)) << 4) + (g & 1) * 8) = pk; }
    { float x0, x1, x2, x3; ARD(x0, 52); ARD(x1, 53); ARD(x2, 54); ARD(x3, 55); u32x2 pk = {cvtpk(x0 * inv, x1 * inv), cvtpk(x2 * inv, x3 * inv)};
      *(__attribute__((address_space(3))) u32x2*)(ost + (0 + c) * 512 + (((26 + (g >> 1)) ^ (c & 15)) << 4) + (g & 1) * 8) = pk; }
    { float x0, x1, x2, x3; ARD(x0, 56); ARD(x1, 57); ARD(x2, 58); ARD(x3, 59); u32x2 pk = {cvtpk(x0 * inv, x1 * inv), cvtpk(x2 * inv, x3 * inv)};
      *(__attribute__((address_space(3))) u32x2*)(ost + (0 + c) * 512 + (((28 + (g >> 1)) ^ (c & 15)) << 4) + (g & 1) * 8) = pk; }
    { float x0, x1, x2, x3; ARD(x0, 60); ARD(x1, 61); ARD(x2, 62); ARD(x3, 63); u32x2 pk = {cvtpk(x0 * inv, x1 * inv), cvtpk(x2 * inv, x3 * inv)};
      *(__attribute__((address_space(3))) u32x2*)(ost + (0 + c) * 512 + (((30 + (g >> 1)) ^ (c & 15)) << 4) + (g & 1) * 8) = pk; }
  }
  { const float inv = __builtin_amdgcn_rcpf(xadd32(xadd16(l[1])));
    { float x0, x1, x2, x3; ARD(x0, 64); ARD(x1, 65); ARD(x2, 66); ARD(x3, 67); u32x2 pk = {cvtpk(x0 * inv, x1 * inv), cvtpk(x2 * inv, x3 * inv)};
      *(__attribute__((address_space(3))) u32x2*)(ost + (16 + c) * 512 + (((0 + (g >> 1)) ^ (c & 15)) << 4) + (g & 1) * 8) = pk; }
    { float x0, x1, x2, x3; ARD(x0, 68); ARD(x1, 69); ARD(x2, 70); ARD(x3, 71); u32x2 pk = {cvtpk(x0 * inv, x1 * inv), cvtpk(x2 * inv, x3 * inv)};
      *(__attribute__((address_space(3))) u32x2*)(ost + (16 + c) * 512 + (((2 + (g >> 1)) ^ (c & 15)) << 4) + (g & 1) * 8) = pk; }
    { float x0, x1, x2, x3; ARD(x0, 72); ARD(x1, 73); ARD(x2, 74); ARD(x3, 75); u32x2 pk = {cvtpk(x0 * inv, x1 * inv), cvtpk(x2 * inv, x3 * inv)};
      *(__attribute__((address_space(3))) u32x2*)(ost + (16 + c) * 512 + (((4 + (g >> 1)) ^ (c & 15)) << 4) + (g & 1) * 8) = pk; }
    { float x0, x1, x2, x3; ARD(x0, 76); ARD(x1, 77); ARD(x2, 78); ARD(x3, 79); u32x2 pk = {cvtpk(x0 * inv, x1 * inv), cvtpk(x2 * inv, x3 * inv)};
      *(__attribute__((address_space(3))) u32x2*)(ost + (16 + c) * 512 + (((6 + (g >> 1)) ^ (c & 15)) << 4) + (g & 1) * 8) = pk; }
    { float x0, x1, x2, x3; ARD(x0, 80); ARD(x1, 81); ARD(x2, 82); ARD(x3, 83); u32x2 pk = {cvtpk(x0 * inv, x1 * inv), cvtpk(x2 * inv, x3 * inv)};
      *(__attribute__((address_space(3))) u32x2*)(ost + (16 + c) * 512 + (((8 + (g >> 1)) ^ (c & 15)) << 4) + (g & 1) * 8) = pk; }
    { float x0, x1, x2, x3; ARD(x0, 84); ARD(x1, 85); ARD(x2, 86); ARD(x3, 87); u32x2 pk = {cvtpk(x0 * inv, x1 * inv), cvtpk(x2 * inv, x3 * inv)};
      *(__attribute__((address_space(3))) u32x2*)(ost + (16 + c) * 512 + (((10 + (g >> 1)) ^ (c & 15)) << 4) + (g & 1) * 8) = pk; }
    { float x0, x1, x2, x3; ARD(x0, 88); ARD(x1, 89); ARD(x2, 90); ARD(x3, 91); u32x2 pk = {cvtpk(x0 * inv, x1 * inv), cvtpk(x2 * inv, x3 * inv)};
      *(__attribute__((address_space(3))) u32x2*)(ost + (16 + c) * 512 + (((12 + (g >> 1)) ^ (c & 15)) << 4) + (g & 1) * 8) = pk; }
    { float x0, x1, x2, x3; ARD(x0, 92); ARD(x1, 93); ARD(x2, 94); ARD(x3, 95); u32x2 pk = {cvtpk(x0 * inv, x1 * inv), cvtpk(x2 * inv, x3 * inv)};
      *(__attribute__((address_space(3))) u32x2*)(ost + (16 + c) * 512 + (((14 + (g >> 1)) ^ (c & 15)) << 4) + (g & 1) * 8) = pk; }
    { float x0, x1, x2, x3; ARD(x0, 96); ARD(x1, 97); ARD(x2, 98); ARD(x3, 99); u32x2 pk = {cvtpk(x0 * inv, x1 * inv), cvtpk(x2 * inv, x3 * inv)};
      *(__attribute__((address_space(3))) u32x2*)(ost + (16 + c) * 512 + (((16 + (g >> 1)) ^ (c & 15)) << 4) + (g & 1) * 8) = pk; }
    { float x0, x1, x2, x3; ARD(x0, 100); ARD(x1, 101); ARD(x2, 102); ARD(x3, 103); u32x2 pk = {cvtpk(x0 * inv, x1 * inv), cvtpk(x2 * inv, x3 * inv)};
      *(__attribute__((address_space(3))) u32x2*)(ost + (16 + c) * 512 + (((18 + (g >> 1)) ^ (c & 15)) << 4) + (g & 1) * 8) = pk; }
    { float x0, x1, x2, x3; ARD(x0, 104); ARD(x1, 105); ARD(x2, 106); ARD(x3, 107); u32x2 pk = {cvtpk(x0 * inv, x1 * inv), cvtpk(x2 * inv, x3 * inv)};
      *(__attribute__((address_space(3))) u32x2*)(ost + (16 + c) * 512 + (((20 + (g >> 1)) ^ (c & 15)) << 4) + (g & 1) * 8) = pk; }
    { float x0, x1, x2, x3; ARD(x0, 108); ARD(x1, 109); ARD(x2, 110); ARD(x3, 111); u32x2 pk = {cvtpk(x0 * inv, x1 * inv), cvtpk(x2 * inv, x3 * inv)};
      *(__attribute__((address_space(3))) u32x2*)(ost + (16 + c) * 512 + (((22 + (g >> 1)) ^ (c & 15)) << 4) + (g & 1) * 8) = pk; }
    { float x0, x1, x2, x3; ARD(x0, 112); ARD(x1, 113); ARD(x2, 114); ARD(x3, 115); u32x2 pk = {cvtpk(x0 * inv, x1 * inv), cvtpk(x2 * inv, x3 * inv)};
      *(__attribute__((address_space(3))) u32x2*)(ost + (16 + c) * 512 + (((24 + (g >> 1)) ^ (c & 15)) << 4) + (g & 1) * 8) = pk; }
    { float x0, x1, x2, x3; ARD(x0, 116); ARD(x1, 117); ARD(x2, 118); ARD(x3, 119); u32x2 pk = {cvtpk(x0 * inv, x1 * inv), cvtpk(x2 * inv, x3 * inv)};
      *(__attribute__((address_space(3))) u32x2*)(ost + (16 + c) * 512 + (((26 + (g >> 1)) ^ (c & 15)) << 4) + (g & 1) * 8) = pk; }
    { float x0, x1, x2, x3; ARD(x0, 120); ARD(x1, 121); ARD(x2, 122); ARD(x3, 123); u32x2 pk = {cvtpk(x0 * inv, x1 * inv), cvtpk(x2 * inv, x3 * inv)};
      *(__attribute__((address_space(3))) u32x2*)(ost + (16 + c) * 512 + (((28 + (g >> 1)) ^ (c & 15)) << 4) + (g & 1) * 8) = pk; }
    { float x0, x1, x2, x3; ARD(x0, 124); ARD(x1, 125); ARD(x2, 126); ARD(x3, 127); u32x2 pk = {cvtpk(x0 * inv, x1 * inv), cvtpk(x2 * inv, x3 * inv)};
      *(__attribute__((address_space(3))) u32x2*)(ost + (16 + c) * 512 + (((30 + (g >> 1)) ^ (c & 15)) << 4) + (g & 1) * 8) = pk; }
  }
  { const float inv = __builtin_amdgcn_rcpf(xadd32(xadd16(l[2])));
    { float x0, x1, x2, x3; ARD(x0, 128); ARD(x1, 129); ARD(x2, 130); ARD(x3, 131); u32x2 pk = {cvtpk(x0 * inv, x1 * inv), cvtpk(x2 * inv, x3 * inv)};
      *(__attribute__((address_space(3))) u32x2*)(ost + (32 + c) * 512 + (((0 + (g >> 1)) ^ (c & 15)) << 4) + (g & 1) * 8) = pk; }
    { float x0, x1, x2, x3; ARD(x0, 132); ARD(x1, 133); ARD(x2, 134); ARD(x3, 135); u32x2 pk = {cvtpk(x0 * inv, x1 * inv), cvtpk(x2 * inv, x3 * inv)};
      *(__attribute__((address_space(3))) u32x2*)(ost + (32 + c) * 512 + (((2 + (g >> 1)) ^ (c & 15)) << 4) + (g & 1) * 8) = pk; }
    { float x0, x1, x2, x3; ARD(x0, 136); ARD(x1, 137); ARD(x2, 138); ARD(x3, 139); u32x2 pk = {cvtpk(x0 * inv, x1 * inv), cvtpk(x2 * inv, x3 * inv)};
      *(__attribute__((address_space(3))) u32x2*)(ost + (32 + c) * 512 + (((4 + (g >> 1)) ^ (c & 15)) << 4) + (g & 1) * 8) = pk; }
    { float x0, x1, x2, x3; ARD(x0, 140); ARD(x1, 141); ARD(x2, 142); ARD(x3, 143); u32x2 pk = {cvtpk(x0 * inv, x1 * inv), cvtpk(x2 * inv, x3 * inv)};
      *(__attribute__((address_space(3))) u32x2*)(ost + (32 + c) * 512 + (((6 + (g >> 1)) ^ (c & 15)) << 4) + (g & 1) * 8) = pk; }
    { float x0, x1, x2, x3; ARD(x0, 144); ARD(x1, 145); ARD(x2, 146); ARD(x3, 147); u32x2 pk = {cvtpk(x0 * inv, x1 * inv), cvtpk(x2 * inv, x3 * inv)};
      *(__attribute__((address_space(3))) u32x2*)(ost + (32 + c) * 512 + (((8 + (g >> 1)) ^ (c & 15)) << 4) + (g & 1) * 8) = pk; }
    { float x0, x1, x2, x3; ARD(x0, 148); ARD(x1, 149); ARD(x2, 150); ARD(x3, 151); u32x2 pk = {cvtpk(x0 * inv, x1 * inv), cvtpk(x2 * inv, x3 * inv)};
      *(__attribute__((address_space(3))) u32x2*)(ost + (32 + c) * 512 + (((10 + (g >> 1)) ^ (c & 15)) << 4) + (g & 1) * 8) = pk; }
    { float x0, x1, x2, x3; ARD(x0, 152); ARD(x1, 153); ARD(x2, 154); ARD(x3, 155); u32x2 pk = {cvtpk(x0 * inv, x1 * inv), cvtpk(x2 * inv, x3 * inv)};
      *(__attribute__((address_space(3))) u32x2*)(ost + (32 + c) * 512 + (((12 + (g >> 1)) ^ (c & 15)) << 4) + (g & 1) * 8) = pk; }
    { float x0, x1, x2, x3; ARD(x0, 156); ARD(x1, 157); ARD(x2, 158); ARD(x3, 159); u32x2 pk = {cvtpk(x0 * inv, x1 * inv), cvtpk(x2 * inv, x3 * inv)};
      *(__attribute__((address_space(3))) u32x2*)(ost + (32 + c) * 512 + (((14 + (g >> 1)) ^ (c & 15)) << 4) + (g & 1) * 8) = pk; }
    { float x0, x1, x2, x3; ARD(x0, 160); ARD(x1, 161); ARD(x2, 162); ARD(x3, 163); u32x2 pk = {cvtpk(x0 * inv, x1 * inv), cvtpk(x2 * inv, x3 * inv)};
      *(__attribute__((address_space(3))) u32x2*)(ost + (32 + c) * 512 + (((16 + (g >> 1)) ^ (c & 15)) << 4) + (g & 1) * 8) = pk; }
    { float x0, x1, x2, x3; ARD(x0, 164); ARD(x1, 165); ARD(x2, 166); ARD(x3, 167); u32x2 pk = {cvtpk(x0 * inv, x1 * inv), cvtpk(x2 * inv, x3 * inv)};
      *(__attribute__((address_space(3))) u32x2*)(ost + (32 + c) * 512 + (((18 + (g >> 1)) ^ (c & 15)) << 4) + (g & 1) * 8) = pk; }
    { float x0, x1, x2, x3; ARD(x0, 168); ARD(x1, 169); ARD(x2, 170); ARD(x3, 171); u32x2 pk = {cvtpk(x0 * inv, x1 * inv), cvtpk(x2 * inv, x3 * inv)};
      *(__attribute__((address_space(3))) u32x2*)(ost + (32 + c) * 512 + (((20 + (g >> 1)) ^ (c & 15)) << 4) + (g & 1) * 8) = pk; }
    { float x0, x1, x2, x3; ARD(x0, 172); ARD(x1, 173); ARD(x2, 174); ARD(x3, 175); u32x2 pk = {cvtpk(x0 * inv, x1 * inv), cvtpk(x2 * inv, x3 * inv)};
      *(__attribute__((address_space(3))) u32x2*)(ost + (32 + c) * 512 + (((22 + (g >> 1)) ^ (c & 15)) << 4) + (g & 1) * 8) = pk; }
    { float x0, x1, x2, x3; ARD(x0, 176); ARD(x1, 177); ARD(x2, 178); ARD(x3, 179); u32x2 pk = {cvtpk(x0 * inv, x1 * inv), cvtpk(x2 * inv, x3 * inv)};
      *(__attribute__((address_space(3))) u32x2*)(ost + (32 + c) * 512 + (((24 + (g >> 1)) ^ (c & 15)) << 4) + (g & 1) * 8) = pk; }
    { float x0, x1, x2, x3; ARD(x0, 180); ARD(x1, 181); ARD(x2, 182); ARD(x3, 183); u32x2 pk = {cvtpk(x0 * inv, x1 * inv), cvtpk(x2 * inv, x3 * inv)};
      *(__attribute__((address_space(3))) u32x2*)(ost + (32 + c) * 512 + (((26 + (g >> 1)) ^ (c & 15)) << 4) + (g & 1) * 8) = pk; }
    { float x0, x1, x2, x3; ARD(x0, 184); ARD(x1, 185); ARD(x2, 186); ARD(x3, 187); u32x2 pk = {cvtpk(x0 * inv, x1 * inv), cvtpk(x2 * inv, x3 * inv)};
      *(__attribute__((address_space(3))) u32x2*)(ost + (32 + c) * 512 + (((28 + (g >> 1)) ^ (c & 15)) << 4) + (g & 1) * 8) = pk; }
    { float x0, x1, x2, x3; ARD(x0, 188); ARD(x1, 189); ARD(x2, 190); ARD(x3, 191); u32x2 pk = {cvtpk(x0 * inv, x1 * inv), cvtpk(x2 * inv, x3 * inv)};
      *(__attribute__((address_space(3))) u32x2*)(ost + (32 + c) * 512 + (((30 + (g >> 1)) ^ (c & 15)) << 4) + (g & 1) * 8) = pk; }
  }
    asm volatile("s_waitcnt lgkmcnt(0)" ::: "memory");
    char* obase = (char*)O + (size_t)qrow0 * 512;
#pragma unroll
    for (int i = 0; i < 24; ++i) { const int row = 2 * i + (lane >> 5), ch = lane & 31;
      u32x4 x = *(__attribute__((address_space(3))) u32x4*)(ost + row * 512 + ((ch ^ (row & 15)) << 4));
      *(u32x4*)(obase + row * 512 + ch * 16) = x; }
  }
}

extern "C" void kernel_launch(void* const* d_in, const int* in_sizes, int n_in,
                              void* d_out, int out_size, void* d_ws, size_t ws_size,
                              hipStream_t stream) {
  const __hip_bfloat16* q = (const __hip_bfloat16*)d_in[0];
  const __hip_bfloat16* k = (const __hip_bfloat16*)d_in[1];
  const __hip_bfloat16* v = (const __hip_bfloat16*)d_in[2];
  __hip_bfloat16* out = (__hip_bfloat16*)d_out;
  (void)hipFuncSetAttribute((const void*)attn_fwd_d256, hipFuncAttributeMaxDynamicSharedMemorySize, LDS_BYTES);
  attn_fwd_d256<<<256, 256, LDS_BYTES, stream>>>(q, k, v, out);
}
```
